```python
import torch

def _softmax(x, axis=-1):
    return torch.softmax(x, dim=axis)
def _gelu(x):
    return torch.nn.functional.gelu(x, approximate="tanh")
def _silu(x):
    return torch.nn.functional.silu(x)
def _sigmoid(x):
    return torch.sigmoid(x)
def _layernorm(x, axis=-1, eps=1e-5):
    m = x.mean(dim=axis, keepdim=True)
    v = x.var(dim=axis, keepdim=True, unbiased=False)
    return (x - m) / torch.sqrt(v + eps)
def _scatter_add(tgt, idx, src, axis=0):
    return tgt.clone().index_add_(axis, idx, src)
def _segment_sum(src, seg, n):
    out = torch.zeros((n,) + src.shape[1:], dtype=src.dtype)
    return out.index_add_(0, seg, src)
def _pad(x, pw):
    flat = tuple(v for pair in reversed(pw) for v in pair)
    return torch.nn.functional.pad(x, flat)

kernel_name = 'standalone__mha_softmax__near_ridge'

def setup_inputs():

    _w = (lambda a: (lambda t: t.to(torch.bfloat16) if t.is_floating_point() else t)(torch.as_tensor(a)))
    B, H, Sq, Sk, D = 8, 32, 368, 1024, 128
    return {
        "q": _w((lambda *s: torch.randn(*s, dtype=torch.bfloat16) * 0.8)(B, H, Sq, D)),
        "k": _w((lambda *s: torch.randn(*s, dtype=torch.bfloat16) * 0.8)(B, H, Sk, D)),
        "v": _w((lambda *s: torch.randn(*s, dtype=torch.bfloat16) * 0.05)(B, H, Sk, D)),
    }

def reference(q, k, v):
    scale = q.shape[-1] ** -0.5
    scores = torch.einsum("bhqd,bhkd->bhqk", q, k) * scale
    p = _softmax(scores, axis=-1)
    return torch.einsum("bhqk,bhkd->bhqd", p, v)
```

```cpp
#include <hip/hip_runtime.h>
#include <hip/hip_bf16.h>
using bf16 = __hip_bfloat16;
constexpr int   D = 128, NW = 8, QBLK = 32, KVBLK = 64;
constexpr float SCALE = 0.088388347648318440f;
constexpr float THR = 8.f;
constexpr int SQ = 368, SK = 1024;
constexpr size_t SHM_V = KVBLK * D * 2, SHM_K = KVBLK * D * 2, SHM_ATTN = 2 * SHM_V + 2 * SHM_K + NW * 64 * 4;
using bf16x8 = __attribute__((ext_vector_type(8))) short;
using s16x4  = __attribute__((ext_vector_type(4))) short;
using f32x16 = __attribute__((ext_vector_type(16))) float;
using u32x4  = __attribute__((ext_vector_type(4))) unsigned;
#define KSWZ(row, colB) ((row) * 256 + ((colB) ^ (((row) & 7) << 4)))
#define SBAR() __builtin_amdgcn_sched_barrier(0)
__device__ __forceinline__ int crow(int r, int hi) { return (r & 3) + 8 * (r >> 2) + 4 * hi; }
__device__ __forceinline__ unsigned cvtpk(float lo, float hi) {
  unsigned r; asm volatile("v_cvt_pk_bf16_f32 %0, %1, %2" : "=v"(r) : "v"(lo), "v"(hi)); return r;
}
__device__ __forceinline__ bf16x8 ld8(const bf16* p) { return *reinterpret_cast<const bf16x8*>(p); }
__device__ __forceinline__ void partialSM(f32x16& p0, f32x16& p1, float& m_reg, float& mn, float& alpha) {
  constexpr float C = SCALE * 1.4426950408889634f;
  float pmax = p0[0]; for (int r = 1; r < 16; ++r) pmax = fmaxf(pmax, p0[r]); for (int r = 0; r < 16; ++r) pmax = fmaxf(pmax, p1[r]);
  { auto rr = __builtin_amdgcn_permlane32_swap(__float_as_uint(pmax), __float_as_uint(pmax), false, false);
    pmax = fmaxf(__uint_as_float(rr[0]), __uint_as_float(rr[1])); }
  if (__builtin_expect(__all(pmax - m_reg <= THR / SCALE), 1)) { mn = m_reg; alpha = 1.f; }
  else { mn = fmaxf(m_reg, pmax); alpha = __builtin_amdgcn_exp2f((m_reg - mn) * C); m_reg = mn; }
  float mnC = -mn * C;
  for (int r = 0; r < 16; ++r) p0[r] = fmaf(p0[r], C, mnC); for (int r = 0; r < 16; ++r) p1[r] = fmaf(p1[r], C, mnC);
  for (int r = 0; r < 16; ++r) p0[r] = __builtin_amdgcn_exp2f(p0[r]);
}
__device__ __forceinline__ void finishSM(f32x16& p0, f32x16& p1, float alpha, float& l_reg, bf16x8& pa0, bf16x8& pa1, bf16x8& pa2, bf16x8& pa3) {
  for (int r = 0; r < 16; ++r) p1[r] = __builtin_amdgcn_exp2f(p1[r]);
  float ps = 0; for (int r = 0; r < 16; ++r) ps += p0[r]; for (int r = 0; r < 16; ++r) ps += p1[r];
  { auto rr = __builtin_amdgcn_permlane32_swap(__float_as_uint(ps), __float_as_uint(ps), false, false);
    ps = __uint_as_float(rr[0]) + __uint_as_float(rr[1]); }
  l_reg = l_reg * alpha + ps;
#define PK4(P, BASE, OUT) do { unsigned a0 = cvtpk(P[BASE + 0], P[BASE + 1]), a1 = cvtpk(P[BASE + 2], P[BASE + 3]);   \
    unsigned b0 = cvtpk(P[BASE + 4], P[BASE + 5]), b1 = cvtpk(P[BASE + 6], P[BASE + 7]);                              \
    auto r0 = __builtin_amdgcn_permlane32_swap(a0, b0, false, false); auto r1 = __builtin_amdgcn_permlane32_swap(a1, b1, false, false); \
    u32x4 w = {r0[0], r1[0], r0[1], r1[1]}; OUT = *reinterpret_cast<bf16x8*>(&w); } while (0)
  PK4(p0, 0, pa0); PK4(p0, 8, pa1); PK4(p1, 0, pa2); PK4(p1, 8, pa3);
#undef PK4
}
__device__ __forceinline__ void qkt(f32x16& p0, f32x16& p1, const bf16* Ks, const bf16x8* qr, int r32, int hi) {
  p0 = f32x16{}; p1 = f32x16{};
  for (int d0 = 0; d0 < 8; ++d0) { int cb = (d0 * 16 + hi * 8) * 2;
    bf16x8 b0 = *reinterpret_cast<const bf16x8*>((const char*)Ks + KSWZ(r32, cb));
    bf16x8 b1 = *reinterpret_cast<const bf16x8*>((const char*)Ks + KSWZ(32 + r32, cb));
    p0 = __builtin_amdgcn_mfma_f32_32x32x16_bf16(b0, qr[d0], p0, 0, 0, 0);
    p1 = __builtin_amdgcn_mfma_f32_32x32x16_bf16(b1, qr[d0], p1, 0, 0, 0); }
}
__device__ __forceinline__ int v_st(int k, int c) { const int kk = (k & ~0xC) | ((k & 4) << 1) | ((k & 8) >> 1); return ((kk >> 3) * 4 + (c >> 5)) * 512 + ((kk & 7) * 32 + (c & 31)) * 2; }
__device__ __forceinline__ int v_rd_base(int lane) { return ((lane & 3) << 3) | (((lane >> 2) & 3) << 6) | (((lane >> 4) & 1) << 5) | (((lane >> 5) & 1) << 8); }
constexpr int v_rd_off(int d0, int ks, int half) { return d0 * 512 + ks * 4096 + half * 2048; }
template <int OFF> __device__ __forceinline__ s16x4 tr_read(int vb) {
  s16x4 r; asm volatile("ds_read_b64_tr_b16 %0, %1 offset:%2" : "=&v"(r) : "v"(vb), "i"(OFF) : "memory"); return r;
}
template <int D0> __device__ __forceinline__ void pv_one(f32x16& od, int vb, bf16x8 pa0, bf16x8 pa1, bf16x8 pa2, bf16x8 pa3) {
  const s16x4 l0 = tr_read<v_rd_off(D0, 0, 0)>(vb), h0 = tr_read<v_rd_off(D0, 0, 1)>(vb), l1 = tr_read<v_rd_off(D0, 1, 0)>(vb), h1 = tr_read<v_rd_off(D0, 1, 1)>(vb);
  const s16x4 l2 = tr_read<v_rd_off(D0, 2, 0)>(vb), h2 = tr_read<v_rd_off(D0, 2, 1)>(vb), l3 = tr_read<v_rd_off(D0, 3, 0)>(vb), h3 = tr_read<v_rd_off(D0, 3, 1)>(vb);
  asm volatile("s_waitcnt lgkmcnt(0)" ::: "memory"); SBAR();
#define PK(L, H) (bf16x8){L[0], L[1], L[2], L[3], H[0], H[1], H[2], H[3]}
  od = __builtin_amdgcn_mfma_f32_32x32x16_bf16(pa0, PK(l0, h0), od, 0, 0, 0);
  od = __builtin_amdgcn_mfma_f32_32x32x16_bf16(pa1, PK(l1, h1), od, 0, 0, 0);
  od = __builtin_amdgcn_mfma_f32_32x32x16_bf16(pa2, PK(l2, h2), od, 0, 0, 0);
  od = __builtin_amdgcn_mfma_f32_32x32x16_bf16(pa3, PK(l3, h3), od, 0, 0, 0);
#undef PK
}
__device__ __forceinline__ void pv_d0(f32x16* o, int vb, bf16x8 pa0, bf16x8 pa1, bf16x8 pa2, bf16x8 pa3) {
  pv_one<0>(o[0], vb, pa0, pa1, pa2, pa3); pv_one<1>(o[1], vb, pa0, pa1, pa2, pa3); pv_one<2>(o[2], vb, pa0, pa1, pa2, pa3); pv_one<3>(o[3], vb, pa0, pa1, pa2, pa3);
}
__global__ __launch_bounds__(64 * NW, 1)
void attn_fwd_dense(const bf16* __restrict__ Q, const bf16* __restrict__ K, const bf16* __restrict__ V, bf16* __restrict__ O) {
  extern __shared__ __attribute__((aligned(16))) char lds[];
  const long bh = blockIdx.y; const int i0 = blockIdx.x * (QBLK * NW);
  const bf16* Qb = Q + bh * SQ * D; const bf16* Kh = K + bh * SK * D; const bf16* Vh = V + bh * SK * D; bf16* Ob = O + bh * SQ * D;
  const int seq = SK; constexpr int LDK = D;
  const int tid = threadIdx.x, wid = tid >> 6, lane = tid & 63, r32 = lane & 31, hi = lane >> 5;
  bf16* V_lds = (bf16*)lds; bf16* K_lds = (bf16*)(lds + 2 * SHM_V);
  float* ws = (float*)(lds + 2 * SHM_V + 2 * SHM_K) + wid * 64; float* li_l = ws; float* al_l = ws + 32;
  float m_reg = -1e30f, l_reg = 0; f32x16 o[4] = {}; bf16x8 qr[8];
  int qrow = i0 + wid * QBLK + r32; if (qrow > SQ - 1) qrow = SQ - 1;
  const bf16* Qw = Qb + (long)qrow * D + hi * 8;
#pragma unroll
  for (int d0 = 0; d0 < 8; ++d0) qr[d0] = ld8(Qw + d0 * 16);
  const int sr = tid >> 4, sc = (tid & 15) * 8, vst0 = v_st(sr, sc), vst1 = v_st(32 + sr, sc);
  const int vb0 = (int)(uintptr_t)V_lds + v_rd_base(lane);
  struct { bf16x8 vs0, vs1, ks0, ks1; } sr_[2];
#define SLOAD(i, k0) do { sr_[i].vs0 = ld8(&Vh[(long)((k0) + sr) * LDK + sc]); sr_[i].vs1 = ld8(&Vh[(long)((k0) + 32 + sr) * LDK + sc]); \
    sr_[i].ks0 = ld8(&Kh[(long)((k0) + sr) * LDK + sc]); sr_[i].ks1 = ld8(&Kh[(long)((k0) + 32 + sr) * LDK + sc]); } while (0)
#define SWRITE(b, i) do { *(bf16x8*)((char*)V_lds + (b) * SHM_V + vst0) = (sr_[i].vs0);          \
    *(bf16x8*)((char*)V_lds + (b) * SHM_V + vst1) = (sr_[i].vs1); int kc = sc * 2;               \
    *(bf16x8*)((char*)K_lds + (b) * SHM_K + KSWZ(sr, kc)) = (sr_[i].ks0);                       \
    *(bf16x8*)((char*)K_lds + (b) * SHM_K + KSWZ(32 + sr, kc)) = (sr_[i].ks1); } while (0)
#define SWAIT() do { asm volatile("s_waitcnt vmcnt(4)" ::: "memory"); } while (0)
#define RESC(a) do { if (__any((a) < 1.f)) { if (hi == 0) al_l[r32] = (a); asm volatile("s_waitcnt lgkmcnt(0)" ::: "memory"); \
    for (int d = 0; d < 4; ++d) for (int r = 0; r < 16; ++r) o[d][r] *= al_l[crow(r, hi)]; } } while (0)
  f32x16 pA0, pA1, pB0, pB1; float mnA, mnB, alA, alB; bf16x8 pa0, pa1, pa2, pa3; const int NT = seq / KVBLK;
  constexpr int SE = 0, SO = 1;
  SLOAD(SE, 0); asm volatile("s_waitcnt vmcnt(0)" ::: "memory"); SWRITE(0, SE); __syncthreads();
  qkt(pA0, pA1, K_lds, qr, r32, hi); partialSM(pA0, pA1, m_reg, mnA, alA);
  SLOAD(SO, KVBLK); if (2 < NT) SLOAD(SE, 2 * KVBLK);
  SWAIT(); SWRITE(1, SO); __syncthreads();
  for (int j = 1; j + 1 < NT; j += 2) {
    SBAR(); qkt(pB0, pB1, (bf16*)((char*)K_lds + SHM_K), qr, r32, hi);
    finishSM(pA0, pA1, alA, l_reg, pa0, pa1, pa2, pa3); SBAR();
    SLOAD(SO, (j + 2) * KVBLK); SBAR();
    pv_d0(o, vb0, pa0, pa1, pa2, pa3); partialSM(pB0, pB1, m_reg, mnB, alB);
    __syncthreads(); SWAIT(); SWRITE(0, SE);
    RESC(alB); __syncthreads();
    SBAR(); qkt(pA0, pA1, K_lds, qr, r32, hi);
    finishSM(pB0, pB1, alB, l_reg, pa0, pa1, pa2, pa3); SBAR();
    if (j + 3 < NT) SLOAD(SE, (j + 1 + 2) * KVBLK); SBAR();
    pv_d0(o, vb0 + (int)SHM_V, pa0, pa1, pa2, pa3); partialSM(pA0, pA1, m_reg, mnA, alA);
    __syncthreads(); SWAIT(); SWRITE(1, SO);
    RESC(alA); __syncthreads();
  }
  SBAR(); qkt(pB0, pB1, (bf16*)((char*)K_lds + SHM_K), qr, r32, hi);
  finishSM(pA0, pA1, alA, l_reg, pa0, pa1, pa2, pa3); SBAR();
  pv_d0(o, vb0, pa0, pa1, pa2, pa3); partialSM(pB0, pB1, m_reg, mnB, alB);
  __syncthreads(); RESC(alB);
  finishSM(pB0, pB1, alB, l_reg, pa0, pa1, pa2, pa3); SBAR();
  pv_d0(o, vb0 + (int)SHM_V, pa0, pa1, pa2, pa3);
  if (hi == 0) li_l[r32] = l_reg; asm volatile("s_waitcnt lgkmcnt(0)" ::: "memory");
  float rli[16];
#pragma unroll
  for (int r = 0; r < 16; ++r) rli[r] = __builtin_amdgcn_rcpf(li_l[crow(r, hi)]);
#pragma unroll
  for (int r = 0; r < 16; ++r) { int orow = i0 + wid * QBLK + crow(r, hi);
    if (orow < SQ) for (int d0 = 0; d0 < 4; ++d0) Ob[(long)orow * D + d0 * 32 + r32] = __float2bfloat16(o[d0][r] * rli[r]); }
}
extern "C" void kernel_launch(void* const* d_in, const int* in_sizes, int n_in, void* d_out, int out_size, void* d_ws, size_t ws_size, hipStream_t stream) {
  hipFuncSetAttribute((const void*)attn_fwd_dense, hipFuncAttributeMaxDynamicSharedMemorySize, (int)SHM_ATTN);
  hipLaunchKernelGGL(attn_fwd_dense, dim3(2, 256), dim3(64 * NW), SHM_ATTN, stream,
                     (const bf16*)d_in[0], (const bf16*)d_in[1], (const bf16*)d_in[2], (bf16*)d_out);
}
```

```cpp
#include <hip/hip_runtime.h>
#include <hip/hip_bf16.h>
#include <stdint.h>

constexpr int SQ = 368, SK = 1024, HD = 128, NBH = 256;
typedef float f32x16 __attribute__((ext_vector_type(16)));
typedef short bf16x8 __attribute__((ext_vector_type(8)));
typedef short s16x4 __attribute__((ext_vector_type(4)));
typedef unsigned u32x4 __attribute__((ext_vector_type(4)));
typedef unsigned u32x2 __attribute__((ext_vector_type(2)));
typedef float f32x2 __attribute__((ext_vector_type(2)));
typedef __bf16 bf16x2 __attribute__((ext_vector_type(2)));
typedef __attribute__((address_space(3))) char* lds_ptr;

__device__ __forceinline__ unsigned pack_bf16(float lo, float hi) {
  f32x2 v = {lo, hi};
  return __builtin_bit_cast(unsigned, __builtin_convertvector(v, bf16x2));
}

__device__ __forceinline__ void exact_path(const uint16_t* Qh, const uint16_t* Kh, const uint16_t* Vh, uint16_t* Oh,
                                           lds_ptr stage, unsigned wid) {
  const unsigned lane = __builtin_amdgcn_mbcnt_hi(~0u, __builtin_amdgcn_mbcnt_lo(~0u, 0u));
  const unsigned r = lane & 31u, h = lane >> 5;
  const float C = 0.12751743082f;
  const unsigned trbase = (4u * h + ((lane >> 2) & 3u)) * 256u + ((lane >> 4) & 1u) * 32u + (lane & 3u) * 8u;
#pragma unroll 1
  for (unsigned b = 0; b < 3; ++b) {
    const unsigned row = 96u * wid + 32u * b + r, rowc = row < (unsigned)SQ ? row : (unsigned)SQ - 1u;
    bf16x8 q[8];
#pragma unroll
    for (int d0 = 0; d0 < 8; ++d0) q[d0] = *(const bf16x8*)(Qh + rowc * HD + 16 * d0 + 8 * h);
    float m = -__builtin_inff(), l = 0.f;
    f32x16 o[4] = {};
#pragma unroll 1
    for (unsigned j = 0; j < SK / 32; ++j) {
      f32x16 st = {};
#pragma unroll
      for (int d0 = 0; d0 < 8; ++d0)
        st = __builtin_amdgcn_mfma_f32_32x32x16_bf16(*(const bf16x8*)(Kh + (32u * j + r) * HD + 16 * d0 + 8 * h), q[d0], st, 0, 0, 0);
      asm volatile("" ::: "memory");
#pragma unroll
      for (int i = 0; i < 8; ++i) {
        const unsigned kr = 4u * i + (lane >> 4);
        const u32x4 x = *(const u32x4*)(Vh + (32u * j + kr) * HD + (lane & 15u) * 8u);
        *(__attribute__((address_space(3))) u32x4*)(stage + (i >> 2) * 16384 + (kr & 15u) * 256u + (lane & 15u) * 16u) = x;
      }
      asm volatile("s_waitcnt lgkmcnt(0)" ::: "memory");
      float mx = st[0];
#pragma unroll
      for (int e = 1; e < 16; ++e) mx = fmaxf(mx, st[e]);
      mx = fmaxf(mx, __shfl_xor(mx, 32));
      const float mn = fmaxf(m, mx * C), alpha = __builtin_amdgcn_exp2f(m - mn);
      m = mn;
      float ps = 0.f;
#pragma unroll
      for (int e = 0; e < 16; ++e) { st[e] = __builtin_amdgcn_exp2f(fmaf(st[e], C, -mn)); ps += st[e]; }
      l = l * alpha + ps;
#pragma unroll
      for (int ks = 0; ks < 2; ++ks) {
        u32x4 pw;
#pragma unroll
        for (int e = 0; e < 4; ++e) pw[e] = pack_bf16(st[8 * ks + 2 * e], st[8 * ks + 2 * e + 1]);
#pragma unroll
        for (int db = 0; db < 4; ++db) {
          if (ks == 0) o[db] *= alpha;
          typedef __attribute__((address_space(3))) s16x4* trp;
          const s16x4 lo = __builtin_amdgcn_ds_read_tr16_b64_v4i16((trp)(stage + ks * 16384 + trbase + db * 64));
          const s16x4 hi = __builtin_amdgcn_ds_read_tr16_b64_v4i16((trp)(stage + ks * 16384 + trbase + db * 64 + 2048));
          o[db] = __builtin_amdgcn_mfma_f32_32x32x16_bf16(__builtin_shufflevector(lo, hi, 0, 1, 2, 3, 4, 5, 6, 7),
                                                          __builtin_bit_cast(bf16x8, pw), o[db], 0, 0, 0);
        }
      }
      asm volatile("s_waitcnt lgkmcnt(0)" ::: "memory");
    }
    l += __shfl_xor(l, 32);
    const float inv = 1.0f / l;
    if (row < (unsigned)SQ) {
#pragma unroll
      for (int db = 0; db < 4; ++db)
#pragma unroll
        for (int g = 0; g < 4; ++g) {
          u32x2 w = {pack_bf16(o[db][4 * g] * inv, o[db][4 * g + 1] * inv), pack_bf16(o[db][4 * g + 2] * inv, o[db][4 * g + 3] * inv)};
          *(u32x2*)(Oh + row * HD + 32 * db + 8 * g + 4 * h) = w;
        }
    }
  }
}

__global__ __launch_bounds__(256, 1) void attn_fwd_1cu(const uint16_t* __restrict__ Q, const uint16_t* __restrict__ K,
                                                 const uint16_t* __restrict__ V, uint16_t* __restrict__ O) {
  __shared__ __attribute__((aligned(1024))) char lds[163840];
  const unsigned bh = (blockIdx.x & 7u) * 32u + (blockIdx.x >> 3);
  const unsigned wid = __builtin_amdgcn_readfirstlane(threadIdx.x >> 6);
  const unsigned ldsb = (unsigned)(uintptr_t)(lds_ptr)lds;
  const unsigned rot = 0u;
  unsigned flag;
  const uint16_t *Qh, *Kh, *Vh;
  uint16_t* Oh;
  asm volatile(
    "s_mov_b32 s39, m0\n\t"
    "s_load_dwordx8 s[88:95], %[karg], 0x0\n\t"
    "s_mov_b32 s42, 0x40000\n\t"
    "s_mov_b32 s43, 0x20000\n\t"
    "v_mbcnt_lo_u32_b32 v242, -1, 0\n\t"
    "v_mbcnt_hi_u32_b32 v242, -1, v242\n\t"
    "s_getpc_b64 s[80:81]\n\t"
    "LPC_%=:\n\t"
    "v_min_u32_e32 v235, 52, v242\n\t"
    "s_mul_i32 s67, %[wid], 0x1a80\n\t"
    "v_lshlrev_b32_e32 v235, 7, v235\n\t"
    "v_add_u32_e32 v235, s67, v235\n\t"
    "v_min_u32_e32 v236, LEND_%=-LPC_%=-4, v235\n\t"
    "global_load_dword v247, v236, s[80:81]\n\t"
    "s_lshl_b32 s68, %[wid], 11\n\t"
    "v_lshrrev_b32_e32 v243, 4, v242\n\t"
    "v_and_b32_e32 v244, 15, v242\n\t"
    "s_lshl_b32 s69, %[wid], 12\n\t"
    "s_add_u32 s57, s69, %[lds]\n\t"
    "s_add_u32 s56, s69, %[rot]\n\t"
    "v_xor_b32_e32 v235, v244, v243\n\t"
    "v_lshlrev_b32_e32 v235, 4, v235\n\t"
    "v_lshl_add_u32 v88, v243, 8, v235\n\t"
    "s_and_b32 s56, s56, 0x3ffff\n\t"
    "s_mov_b32 m0, s57\n\t"
    "v_xor_b32_e32 v89, 64, v88\n\t"
    "v_xor_b32_e32 v90, 0x80, v88\n\t"
    "v_xor_b32_e32 v91, 0xc0, v88\n\t"
    "s_lshl_b32 s82, %[bh], 18\n\t"
    "s_mul_i32 s83, %[bh], 0x17000\n\t"
    "s_waitcnt lgkmcnt(0)\n\t"
    "s_add_u32 s90, s90, s82\n\t"
    "s_addc_u32 s91, s91, 0\n\t"
    "s_mov_b32 s40, s90\n\t"
    "s_and_b32 s41, s91, 0xffff\n\t"
    "buffer_load_dwordx4 v88, s[40:43], s56 offen offset:0 nt lds\n\t"
    "buffer_load_dwordx4 v89, s[40:43], s56 offen offset:1024 nt lds\n\t"
    "buffer_load_dwordx4 v90, s[40:43], s56 offen offset:2048 nt lds\n\t"
    "buffer_load_dwordx4 v91, s[40:43], s56 offen offset:3072 nt lds\n\t"
    "s_add_u32 s88, s88, s83\n\t"
    "s_addc_u32 s89, s89, 0\n\t"
    "s_mov_b32 s48, s88\n\t"
    "s_and_b32 s49, s89, 0xffff\n\t"
    "s_mov_b32 s50, 0x17000\n\t"
    "s_mov_b32 s51, 0x20000\n\t"
    "s_mul_i32 s71, %[wid], 0x6000\n\t"
    "s_add_u32 m0, s57, 0x10000\n\t"
    "s_add_u32 s67, s71, 0\n\t"
    "buffer_load_dwordx4 v88, s[48:51], s67 offen offset:0 nt lds\n\t"
    "buffer_load_dwordx4 v89, s[48:51], s67 offen offset:1024 nt lds\n\t"
    "buffer_load_dwordx4 v90, s[48:51], s67 offen offset:2048 nt lds\n\t"
    "buffer_load_dwordx4 v91, s[48:51], s67 offen offset:3072 nt lds\n\t"
    "s_add_u32 s92, s92, s82\n\t"
    "s_addc_u32 s93, s93, 0\n\t"
    "s_mov_b32 s44, s92\n\t"
    "s_and_b32 s45, s93, 0xffff\n\t"
    "s_mov_b32 s46, 0x40000\n\t"
    "s_mov_b32 s47, 0x20000\n\t"
    "v_lshlrev_b32_e32 v235, 4, v244\n\t"
    "v_lshlrev_b32_e32 v236, 6, v243\n\t"
    "v_xor_b32_e32 v235, v235, v236\n\t"
    "v_lshl_add_u32 v92, v243, 8, v235\n\t"
    "s_add_u32 m0, s57, 0x4000\n\t"
    "s_add_u32 s75, s68, %[lds]\n\t"
    "buffer_load_dwordx4 v92, s[44:47], s56 offen offset:0 nt lds\n\t"
    "buffer_load_dwordx4 v92, s[44:47], s56 offen offset:1024 nt lds\n\t"
    "buffer_load_dwordx4 v92, s[44:47], s56 offen offset:2048 nt lds\n\t"
    "buffer_load_dwordx4 v92, s[44:47], s56 offen offset:3072 nt lds\n\t"
    "s_add_u32 s56, s68, %[rot]\n\t"
    "s_add_u32 s56, s56, 0x4000\n\t"
    "s_and_b32 s56, s56, 0x3ffff\n\t"
    "s_lshl_b32 s67, %[wid], 7\n\t"
    "s_and_b32 s67, s67, 0x80\n\t"
    "v_xor_b32_e32 v245, s67, v88\n\t"
    "v_xor_b32_e32 v246, 64, v245\n\t"
    "v_xor_b32_e32 v237, v243, v244\n\t"
    "v_lshlrev_b32_e32 v237, 4, v237\n\t"
    "v_lshl_add_u32 v237, v244, 8, v237\n\t"
    "s_add_u32 s67, s69, 0x10000\n\t"
    "v_xor_b32_e32 v76, 0, v237\n\t"
    "v_add_u32_e32 v76, %[lds], v76\n\t"
    "v_add_u32_e32 v224, s67, v76\n\t"
    "v_add_u32_e32 v228, 0x10000, v224\n\t"
    "v_xor_b32_e32 v77, 64, v237\n\t"
    "v_add_u32_e32 v77, %[lds], v77\n\t"
    "v_add_u32_e32 v225, s67, v77\n\t"
    "v_add_u32_e32 v229, 0x10000, v225\n\t"
    "v_xor_b32_e32 v78, 0x80, v237\n\t"
    "v_add_u32_e32 v78, %[lds], v78\n\t"
    "v_add_u32_e32 v226, s67, v78\n\t"
    "v_add_u32_e32 v230, 0x10000, v226\n\t"
    "v_xor_b32_e32 v79, 0xc0, v237\n\t"
    "v_add_u32_e32 v79, %[lds], v79\n\t"
    "v_add_u32_e32 v227, s67, v79\n\t"
    "v_add_u32_e32 v231, 0x10000, v227\n\t"
    "s_add_u32 s67, s57, 0x10000\n\t"
    "v_lshlrev_b32_e32 v84, 4, v242\n\t"
    "v_add_u32_e32 v84, s67, v84\n\t"
    "v_add_u32_e32 v85, 0x10000, v84\n\t"
    "v_mov_b32_e32 v86, v85\n\t"
    "v_lshrrev_b32_e32 v237, 2, v242\n\t"
    "v_and_b32_e32 v238, 3, v237\n\t"
    "v_lshlrev_b32_e32 v238, 6, v238\n\t"
    "v_lshl_add_u32 v238, v237, 8, v238\n\t"
    "v_and_b32_e32 v239, 3, v242\n\t"
    "v_lshl_add_u32 v238, v239, 4, v238\n\t"
    "s_add_u32 s67, %[lds], 0x4000\n\t"
    "v_xor_b32_e32 v80, 0, v238\n\t"
    "v_add_u32_e32 v80, s67, v80\n\t"
    "v_xor_b32_e32 v81, 64, v238\n\t"
    "v_add_u32_e32 v81, s67, v81\n\t"
    "v_xor_b32_e32 v82, 0x80, v238\n\t"
    "v_add_u32_e32 v82, s67, v82\n\t"
    "v_xor_b32_e32 v83, 0xc0, v238\n\t"
    "v_add_u32_e32 v83, s67, v83\n\t"
    "v_bfe_u32 v237, v242, 1, 1\n\t"
    "v_lshrrev_b32_e32 v238, 5, v242\n\t"
    "v_cmp_eq_u32_e32 vcc, v237, v238\n\t"
    "v_mov_b32_e32 v238, 0x3f803f80\n\t"
    "v_and_b32_e32 v237, 1, v242\n\t"
    "v_cndmask_b32_e64 v72, 0, v238, vcc\n\t"
    "v_cndmask_b32_e64 v73, 0, v238, vcc\n\t"
    "v_cndmask_b32_e64 v74, 0, v238, vcc\n\t"
    "v_cndmask_b32_e64 v75, 0, v238, vcc\n\t"
    "v_and_b32_e32 v239, 2, v242\n\t"
    "v_cmp_eq_u32_e32 vcc, 0, v239\n\t"
    "s_nop 1\n\t"
    "v_cndmask_b32_e64 v68, 0, v238, vcc\n\t"
    "v_cndmask_b32_e64 v252, v238, 0, vcc\n\t"
    "v_cndmask_b32_e64 v69, 0, v238, vcc\n\t"
    "v_cndmask_b32_e64 v253, v238, 0, vcc\n\t"
    "v_cndmask_b32_e64 v70, 0, v238, vcc\n\t"
    "v_cndmask_b32_e64 v254, v238, 0, vcc\n\t"
    "v_cndmask_b32_e64 v71, 0, v238, vcc\n\t"
    "v_cndmask_b32_e64 v255, v238, 0, vcc\n\t"
    "v_mul_u32_u24_e32 v93, 0xaaaa, v237\n\t"
    "v_add_u32_e32 v93, 0x4444, v93\n\t"
    "v_lshl_or_b32 v93, v93, 16, v93\n\t"
    "v_mov_b32_e32 v56, 0\n\t"
    "v_mov_b32_e32 v57, 0\n\t"
    "v_mov_b32_e32 v58, 0\n\t"
    "v_mov_b32_e32 v59, 0\n\t"
    "v_mov_b32_e32 v60, 0\n\t"
    "v_mov_b32_e32 v61, 0\n\t"
    "v_mov_b32_e32 v62, 0\n\t"
    "v_mov_b32_e32 v63, 0\n\t"
    "v_mov_b32_e32 v64, 0\n\t"
    "v_mov_b32_e32 v65, 0\n\t"
    "v_mov_b32_e32 v66, 0\n\t"
    "v_mov_b32_e32 v67, 0\n\t"
    "s_add_u32 s94, s94, s83\n\t"
    "s_addc_u32 s95, s95, 0\n\t"
    "s_mov_b32 s52, s94\n\t"
    "s_and_b32 s53, s95, 0xffff\n\t"
    "s_mov_b32 s54, 0x17000\n\t"
    "s_mov_b32 s55, 0x20000\n\t"
    "s_mov_b32 s64, 0x3e0293ee\n\t"
    "s_mov_b32 s65, 0x3e0293ee\n\t"
    "s_mov_b32 s66, 0xffff0000\n\t"
    "s_mov_b32 s70, 0x4e800000\n\t"
    "s_mov_b32 s74, 0x7f7fffff\n\t"
    "s_cmp_eq_u32 %[wid], 3\n\t"
    "s_cselect_b32 s74, 0xf149f2ca, s74\n\t"
    "s_mov_b32 s63, 0xfffe8000\n\t"
    "s_mov_b32 s58, 0\n\t"
    "s_mov_b32 s59, 0x8000\n\t"
    "s_mov_b32 s60, 0x8000\n\t"
    "s_add_u32 m0, s57, 0x14000\n\t"
    "s_add_u32 s67, s71, 0x1000\n\t"
    "buffer_load_dwordx4 v88, s[48:51], s67 offen offset:0 nt lds\n\t"
    "buffer_load_dwordx4 v89, s[48:51], s67 offen offset:1024 nt lds\n\t"
    "buffer_load_dwordx4 v90, s[48:51], s67 offen offset:2048 nt lds\n\t"
    "buffer_load_dwordx4 v91, s[48:51], s67 offen offset:3072 nt lds\n\t"
    "s_waitcnt vmcnt(8)\n\t"
    "s_barrier\n\t"
    "ds_read_b128 v[0:3], v224 offset:0\n\t"
    "ds_read_b128 v[4:7], v225 offset:0\n\t"
    "ds_read_b128 v[8:11], v226 offset:0\n\t"
    "ds_read_b128 v[12:15], v227 offset:0\n\t"
    "ds_read_b128 a[96:99], v76 offset:0\n\t"
    "ds_read_b128 a[100:103], v77 offset:0\n\t"
    "ds_read_b128 a[104:107], v78 offset:0\n\t"
    "ds_read_b128 a[108:111], v79 offset:0\n\t"
    "ds_read_b128 a[112:115], v76 offset:4096\n\t"
    "ds_read_b128 a[116:119], v77 offset:4096\n\t"
    "ds_read_b128 a[120:123], v78 offset:4096\n\t"
    "ds_read_b128 a[124:127], v79 offset:4096\n\t"
    "ds_read_b128 v[192:195], v76 offset:8192\n\t"
    "ds_read_b128 v[196:199], v77 offset:8192\n\t"
    "ds_read_b128 v[200:203], v78 offset:8192\n\t"
    "ds_read_b128 v[204:207], v79 offset:8192\n\t"
    "ds_read_b128 v[208:211], v76 offset:12288\n\t"
    "ds_read_b128 v[212:215], v77 offset:12288\n\t"
    "ds_read_b128 v[216:219], v78 offset:12288\n\t"
    "ds_read_b128 v[220:223], v79 offset:12288\n\t"
    "s_add_u32 m0, s57, 0x18000\n\t"
    "s_add_u32 s67, s71, 0x2000\n\t"
    "buffer_load_dwordx4 v88, s[48:51], s67 offen offset:0 nt lds\n\t"
    "buffer_load_dwordx4 v89, s[48:51], s67 offen offset:1024 nt lds\n\t"
    "buffer_load_dwordx4 v90, s[48:51], s67 offen offset:2048 nt lds\n\t"
    "buffer_load_dwordx4 v91, s[48:51], s67 offen offset:3072 nt lds\n\t"
    "s_waitcnt lgkmcnt(15)\n\t"
    "v_lshlrev_b32_e32 v232, 16, v0\n\t"
    "v_and_b32_e32 v233, s66, v0\n\t"
    "v_lshlrev_b32_e32 v234, 16, v1\n\t"
    "v_and_b32_e32 v235, s66, v1\n\t"
    "v_lshlrev_b32_e32 v236, 16, v2\n\t"
    "v_and_b32_e32 v237, s66, v2\n\t"
    "v_lshlrev_b32_e32 v238, 16, v3\n\t"
    "v_and_b32_e32 v239, s66, v3\n\t"
    "v_pk_mul_f32 v[232:233], v[232:233], s[64:65]\n\t"
    "v_pk_mul_f32 v[234:235], v[234:235], s[64:65]\n\t"
    "v_pk_mul_f32 v[236:237], v[236:237], s[64:65]\n\t"
    "v_pk_mul_f32 v[238:239], v[238:239], s[64:65]\n\t"
    "v_cvt_pk_bf16_f32 v232, v232, v233\n\t"
    "v_cvt_pk_bf16_f32 v234, v234, v235\n\t"
    "v_cvt_pk_bf16_f32 v236, v236, v237\n\t"
    "v_cvt_pk_bf16_f32 v238, v238, v239\n\t"
    "v_accvgpr_write_b32 a0, v232\n\t"
    "v_accvgpr_write_b32 a1, v234\n\t"
    "v_accvgpr_write_b32 a2, v236\n\t"
    "v_accvgpr_write_b32 a3, v238\n\t"
    "s_waitcnt lgkmcnt(0)\n\t"
    "v_lshlrev_b32_e32 v232, 16, v4\n\t"
    "v_and_b32_e32 v233, s66, v4\n\t"
    "v_lshlrev_b32_e32 v234, 16, v5\n\t"
    "v_and_b32_e32 v235, s66, v5\n\t"
    "v_lshlrev_b32_e32 v236, 16, v6\n\t"
    "v_and_b32_e32 v237, s66, v6\n\t"
    "v_lshlrev_b32_e32 v238, 16, v7\n\t"
    "v_and_b32_e32 v239, s66, v7\n\t"
    "v_pk_mul_f32 v[232:233], v[232:233], s[64:65]\n\t"
    "v_pk_mul_f32 v[234:235], v[234:235], s[64:65]\n\t"
    "v_pk_mul_f32 v[236:237], v[236:237], s[64:65]\n\t"
    "v_pk_mul_f32 v[238:239], v[238:239], s[64:65]\n\t"
    "v_mfma_f32_16x16x32_bf16 v[0:3], a[96:99], a[0:3], 0\n\t"
    "v_cvt_pk_bf16_f32 v232, v232, v233\n\t"
    "v_cvt_pk_bf16_f32 v234, v234, v235\n\t"
    "v_cvt_pk_bf16_f32 v236, v236, v237\n\t"
    "v_mfma_f32_16x16x32_bf16 v[4:7], a[112:115], a[0:3], 0\n\t"
    "v_cvt_pk_bf16_f32 v238, v238, v239\n\t"
    "v_accvgpr_write_b32 a4, v232\n\t"
    "v_accvgpr_write_b32 a5, v234\n\t"
    "v_accvgpr_write_b32 a6, v236\n\t"
    "v_accvgpr_write_b32 a7, v238\n\t"
    "v_lshlrev_b32_e32 v232, 16, v8\n\t"
    "v_and_b32_e32 v233, s66, v8\n\t"
    "v_mfma_f32_16x16x32_bf16 v[0:3], a[100:103], a[4:7], v[0:3]\n\t"
    "v_lshlrev_b32_e32 v234, 16, v9\n\t"
    "v_and_b32_e32 v235, s66, v9\n\t"
    "v_mfma_f32_16x16x32_bf16 v[4:7], a[116:119], a[4:7], v[4:7]\n\t"
    "v_lshlrev_b32_e32 v236, 16, v10\n\t"
    "v_and_b32_e32 v237, s66, v10\n\t"
    "v_lshlrev_b32_e32 v238, 16, v11\n\t"
    "v_and_b32_e32 v239, s66, v11\n\t"
    "v_pk_mul_f32 v[232:233], v[232:233], s[64:65]\n\t"
    "v_pk_mul_f32 v[234:235], v[234:235], s[64:65]\n\t"
    "v_pk_mul_f32 v[236:237], v[236:237], s[64:65]\n\t"
    "v_pk_mul_f32 v[238:239], v[238:239], s[64:65]\n\t"
    "v_cvt_pk_bf16_f32 v232, v232, v233\n\t"
    "v_cvt_pk_bf16_f32 v234, v234, v235\n\t"
    "v_cvt_pk_bf16_f32 v236, v236, v237\n\t"
    "v_cvt_pk_bf16_f32 v238, v238, v239\n\t"
    "v_accvgpr_write_b32 a8, v232\n\t"
    "v_accvgpr_write_b32 a9, v234\n\t"
    "v_accvgpr_write_b32 a10, v236\n\t"
    "v_accvgpr_write_b32 a11, v238\n\t"
    "v_lshlrev_b32_e32 v232, 16, v12\n\t"
    "v_and_b32_e32 v233, s66, v12\n\t"
    "v_mfma_f32_16x16x32_bf16 v[0:3], a[104:107], a[8:11], v[0:3]\n\t"
    "v_lshlrev_b32_e32 v234, 16, v13\n\t"
    "v_and_b32_e32 v235, s66, v13\n\t"
    "v_mfma_f32_16x16x32_bf16 v[4:7], a[120:123], a[8:11], v[4:7]\n\t"
    "v_lshlrev_b32_e32 v236, 16, v14\n\t"
    "v_and_b32_e32 v237, s66, v14\n\t"
    "v_lshlrev_b32_e32 v238, 16, v15\n\t"
    "v_and_b32_e32 v239, s66, v15\n\t"
    "v_pk_mul_f32 v[232:233], v[232:233], s[64:65]\n\t"
    "v_pk_mul_f32 v[234:235], v[234:235], s[64:65]\n\t"
    "v_pk_mul_f32 v[236:237], v[236:237], s[64:65]\n\t"
    "v_pk_mul_f32 v[238:239], v[238:239], s[64:65]\n\t"
    "v_cvt_pk_bf16_f32 v232, v232, v233\n\t"
    "v_cvt_pk_bf16_f32 v234, v234, v235\n\t"
    "v_cvt_pk_bf16_f32 v236, v236, v237\n\t"
    "v_cvt_pk_bf16_f32 v238, v238, v239\n\t"
    "v_accvgpr_write_b32 a12, v232\n\t"
    "v_accvgpr_write_b32 a13, v234\n\t"
    "v_accvgpr_write_b32 a14, v236\n\t"
    "v_accvgpr_write_b32 a15, v238\n\t"
    "s_nop 1\n\t"
    "v_mfma_f32_16x16x32_bf16 v[0:3], a[108:111], a[12:15], v[0:3]\n\t"
    "v_mfma_f32_16x16x32_bf16 v[4:7], a[124:127], a[12:15], v[4:7]\n\t"
    "s_nop 6\n\t"
    "v_max3_f32 v240, v0, v1, v2\n\t"
    "v_max3_f32 v240, v240, v3, v4\n\t"
    "v_max3_f32 v240, v240, v5, v6\n\t"
    "v_max_f32_e32 v240, v240, v7\n\t"
    "v_mov_b32_e32 v241, v240\n\t"
    "s_nop 1\n\t"
    "v_permlane32_swap_b32 v240, v241\n\t"
    "v_max_f32_e32 v240, v240, v241\n\t"
    "v_mov_b32_e32 v241, v240\n\t"
    "s_nop 1\n\t"
    "v_permlane16_swap_b32 v240, v241\n\t"
    "v_max_f32_e32 v240, v240, v241\n\t"
    "v_sub_f32_e32 v32, 0, v240\n\t"
    "v_mov_b32_e32 v33, v32\n\t"
    "v_mov_b32_e32 v34, v32\n\t"
    "v_mov_b32_e32 v35, v32\n\t"
    "v_add_f32_e32 v7, v32, v7\n\t"
    "v_add_f32_e32 v6, v32, v6\n\t"
    "v_mfma_f32_16x16x32_bf16 v[8:11], v[192:195], a[0:3], v[32:35]\n\t"
    "v_add_f32_e32 v5, v32, v5\n\t"
    "v_add_f32_e32 v4, v32, v4\n\t"
    "v_mfma_f32_16x16x32_bf16 v[8:11], v[196:199], a[4:7], v[8:11]\n\t"
    "v_add_f32_e32 v3, v32, v3\n\t"
    "v_add_f32_e32 v2, v32, v2\n\t"
    "v_mfma_f32_16x16x32_bf16 v[8:11], v[200:203], a[8:11], v[8:11]\n\t"
    "v_add_f32_e32 v1, v32, v1\n\t"
    "v_add_f32_e32 v0, v32, v0\n\t"
    "v_mfma_f32_16x16x32_bf16 v[8:11], v[204:207], a[12:15], v[8:11]\n\t"
    "v_exp_f32_e32 v7, v7\n\t"
    "v_mfma_f32_16x16x32_bf16 v[12:15], v[208:211], a[0:3], v[32:35]\n\t"
    "v_exp_f32_e32 v6, v6\n\t"
    "v_mfma_f32_16x16x32_bf16 v[12:15], v[212:215], a[4:7], v[12:15]\n\t"
    "v_exp_f32_e32 v5, v5\n\t"
    "v_mfma_f32_16x16x32_bf16 v[12:15], v[216:219], a[8:11], v[12:15]\n\t"
    "v_exp_f32_e32 v4, v4\n\t"
    "v_mfma_f32_16x16x32_bf16 v[12:15], v[220:223], a[12:15], v[12:15]\n\t"
    "v_exp_f32_e32 v3, v3\n\t"
    "v_exp_f32_e32 v2, v2\n\t"
    "v_exp_f32_e32 v1, v1\n\t"
    "v_exp_f32_e32 v0, v0\n\t"
    "s_nop 0\n\t"
    "v_cvt_pk_bf16_f32 v7, v6, v7\n\t"
    "v_cvt_pk_bf16_f32 v6, v4, v5\n\t"
    "v_cvt_pk_bf16_f32 v5, v2, v3\n\t"
    "v_cvt_pk_bf16_f32 v4, v0, v1\n\t"
    "s_waitcnt vmcnt(8)\n\t"
    "s_barrier\n\t"
    "ds_read_b64_tr_b16 a[128:129], v80 offset:0\n\t"
    "ds_read_b64_tr_b16 a[130:131], v80 offset:4096\n\t"
    "ds_read_b64_tr_b16 a[132:133], v80 offset:8\n\t"
    "ds_read_b64_tr_b16 a[134:135], v80 offset:4104\n\t"
    "ds_read_b64_tr_b16 a[136:137], v81 offset:0\n\t"
    "ds_read_b64_tr_b16 a[138:139], v81 offset:4096\n\t"
    "ds_read_b64_tr_b16 a[140:141], v81 offset:8\n\t"
    "ds_read_b64_tr_b16 a[142:143], v81 offset:4104\n\t"
    "ds_read_b64_tr_b16 a[144:145], v82 offset:0\n\t"
    "ds_read_b64_tr_b16 a[146:147], v82 offset:4096\n\t"
    "ds_read_b64_tr_b16 a[148:149], v82 offset:8\n\t"
    "ds_read_b64_tr_b16 a[150:151], v82 offset:4104\n\t"
    "ds_read_b64_tr_b16 a[152:153], v83 offset:0\n\t"
    "ds_read_b64_tr_b16 a[154:155], v83 offset:4096\n\t"
    "ds_read_b64_tr_b16 a[156:157], v83 offset:8\n\t"
    "ds_read_b64_tr_b16 a[158:159], v83 offset:4104\n\t"
    "ds_read_b64_tr_b16 a[160:161], v80 offset:8192\n\t"
    "ds_read_b64_tr_b16 a[162:163], v80 offset:12288\n\t"
    "ds_read_b64_tr_b16 a[164:165], v80 offset:8200\n\t"
    "ds_read_b64_tr_b16 a[166:167], v80 offset:12296\n\t"
    "ds_read_b64_tr_b16 a[168:169], v81 offset:8192\n\t"
    "ds_read_b64_tr_b16 a[170:171], v81 offset:12288\n\t"
    "ds_read_b64_tr_b16 a[172:173], v81 offset:8200\n\t"
    "ds_read_b64_tr_b16 a[174:175], v81 offset:12296\n\t"
    "ds_read_b64_tr_b16 a[176:177], v82 offset:8192\n\t"
    "ds_read_b64_tr_b16 a[178:179], v82 offset:12288\n\t"
    "ds_read_b64_tr_b16 a[180:181], v82 offset:8200\n\t"
    "ds_read_b64_tr_b16 a[182:183], v82 offset:12296\n\t"
    "ds_read_b64_tr_b16 a[184:185], v83 offset:8192\n\t"
    "ds_read_b64_tr_b16 a[186:187], v83 offset:12288\n\t"
    "ds_read_b64_tr_b16 a[188:189], v83 offset:8200\n\t"
    "ds_read_b64_tr_b16 a[190:191], v83 offset:12296\n\t"
    "s_waitcnt vmcnt(4)\n\t"
    "ds_read_b128 v[16:19], v224 offset:16384\n\t"
    "ds_read_b128 v[20:23], v225 offset:16384\n\t"
    "ds_read_b128 v[24:27], v226 offset:16384\n\t"
    "ds_read_b128 v[28:31], v227 offset:16384\n\t"
    "s_waitcnt lgkmcnt(15)\n\t"
    "v_mfma_f32_16x16x32_bf16 v[96:99], a[128:131], v[4:7], 0\n\t"
    "v_exp_f32_e32 v8, v8\n\t"
    "v_mfma_f32_16x16x32_bf16 v[100:103], a[132:135], v[4:7], 0\n\t"
    "v_exp_f32_e32 v9, v9\n\t"
    "v_mfma_f32_16x16x32_bf16 v[104:107], a[136:139], v[4:7], 0\n\t"
    "v_exp_f32_e32 v10, v10\n\t"
    "v_mfma_f32_16x16x32_bf16 v[108:111], a[140:143], v[4:7], 0\n\t"
    "v_exp_f32_e32 v11, v11\n\t"
    "v_mfma_f32_16x16x32_bf16 v[112:115], a[144:147], v[4:7], 0\n\t"
    "v_exp_f32_e32 v12, v12\n\t"
    "v_mfma_f32_16x16x32_bf16 v[116:119], a[148:151], v[4:7], 0\n\t"
    "v_exp_f32_e32 v13, v13\n\t"
    "v_mfma_f32_16x16x32_bf16 v[120:123], a[152:155], v[4:7], 0\n\t"
    "v_exp_f32_e32 v14, v14\n\t"
    "v_mfma_f32_16x16x32_bf16 v[124:127], a[156:159], v[4:7], 0\n\t"
    "v_exp_f32_e32 v15, v15\n\t"
    "s_waitcnt lgkmcnt(0)\n\t"
    "v_cvt_pk_bf16_f32 v8, v8, v9\n\t"
    "v_cvt_pk_bf16_f32 v9, v10, v11\n\t"
    "v_cvt_pk_bf16_f32 v10, v12, v13\n\t"
    "v_cvt_pk_bf16_f32 v11, v14, v15\n\t"
    "v_lshlrev_b32_e32 v232, 16, v16\n\t"
    "v_and_b32_e32 v233, s66, v16\n\t"
    "v_mfma_f32_16x16x32_bf16 v[96:99], a[160:163], v[8:11], v[96:99]\n\t"
    "v_lshlrev_b32_e32 v234, 16, v17\n\t"
    "v_and_b32_e32 v235, s66, v17\n\t"
    "v_mfma_f32_16x16x32_bf16 v[100:103], a[164:167], v[8:11], v[100:103]\n\t"
    "v_lshlrev_b32_e32 v236, 16, v18\n\t"
    "v_and_b32_e32 v237, s66, v18\n\t"
    "v_lshlrev_b32_e32 v238, 16, v19\n\t"
    "v_and_b32_e32 v239, s66, v19\n\t"
    "v_pk_mul_f32 v[232:233], v[232:233], s[64:65]\n\t"
    "v_pk_mul_f32 v[234:235], v[234:235], s[64:65]\n\t"
    "v_pk_mul_f32 v[236:237], v[236:237], s[64:65]\n\t"
    "v_pk_mul_f32 v[238:239], v[238:239], s[64:65]\n\t"
    "v_mfma_f32_16x16x32_bf16 v[104:107], a[168:171], v[8:11], v[104:107]\n\t"
    "v_cvt_pk_bf16_f32 v232, v232, v233\n\t"
    "v_cvt_pk_bf16_f32 v234, v234, v235\n\t"
    "v_cvt_pk_bf16_f32 v236, v236, v237\n\t"
    "v_mfma_f32_16x16x32_bf16 v[108:111], a[172:175], v[8:11], v[108:111]\n\t"
    "v_cvt_pk_bf16_f32 v238, v238, v239\n\t"
    "v_accvgpr_write_b32 a16, v232\n\t"
    "v_accvgpr_write_b32 a17, v234\n\t"
    "v_accvgpr_write_b32 a18, v236\n\t"
    "v_accvgpr_write_b32 a19, v238\n\t"
    "v_lshlrev_b32_e32 v232, 16, v20\n\t"
    "v_and_b32_e32 v233, s66, v20\n\t"
    "v_mfma_f32_16x16x32_bf16 v[112:115], a[176:179], v[8:11], v[112:115]\n\t"
    "v_lshlrev_b32_e32 v234, 16, v21\n\t"
    "v_and_b32_e32 v235, s66, v21\n\t"
    "v_mfma_f32_16x16x32_bf16 v[116:119], a[180:183], v[8:11], v[116:119]\n\t"
    "v_lshlrev_b32_e32 v236, 16, v22\n\t"
    "v_and_b32_e32 v237, s66, v22\n\t"
    "v_lshlrev_b32_e32 v238, 16, v23\n\t"
    "v_and_b32_e32 v239, s66, v23\n\t"
    "v_pk_mul_f32 v[232:233], v[232:233], s[64:65]\n\t"
    "v_pk_mul_f32 v[234:235], v[234:235], s[64:65]\n\t"
    "v_pk_mul_f32 v[236:237], v[236:237], s[64:65]\n\t"
    "v_pk_mul_f32 v[238:239], v[238:239], s[64:65]\n\t"
    "v_mfma_f32_16x16x32_bf16 v[16:19], a[96:99], a[16:19], 0\n\t"
    "v_cvt_pk_bf16_f32 v232, v232, v233\n\t"
    "v_cvt_pk_bf16_f32 v234, v234, v235\n\t"
    "v_cvt_pk_bf16_f32 v236, v236, v237\n\t"
    "v_mfma_f32_16x16x32_bf16 v[20:23], a[112:115], a[16:19], 0\n\t"
    "v_cvt_pk_bf16_f32 v238, v238, v239\n\t"
    "v_accvgpr_write_b32 a20, v232\n\t"
    "v_accvgpr_write_b32 a21, v234\n\t"
    "v_accvgpr_write_b32 a22, v236\n\t"
    "v_accvgpr_write_b32 a23, v238\n\t"
    "v_lshlrev_b32_e32 v232, 16, v24\n\t"
    "v_and_b32_e32 v233, s66, v24\n\t"
    "v_mfma_f32_16x16x32_bf16 v[16:19], a[100:103], a[20:23], v[16:19]\n\t"
    "v_lshlrev_b32_e32 v234, 16, v25\n\t"
    "v_and_b32_e32 v235, s66, v25\n\t"
    "v_mfma_f32_16x16x32_bf16 v[20:23], a[116:119], a[20:23], v[20:23]\n\t"
    "v_lshlrev_b32_e32 v236, 16, v26\n\t"
    "v_and_b32_e32 v237, s66, v26\n\t"
    "v_lshlrev_b32_e32 v238, 16, v27\n\t"
    "v_and_b32_e32 v239, s66, v27\n\t"
    "v_pk_mul_f32 v[232:233], v[232:233], s[64:65]\n\t"
    "v_pk_mul_f32 v[234:235], v[234:235], s[64:65]\n\t"
    "v_pk_mul_f32 v[236:237], v[236:237], s[64:65]\n\t"
    "v_pk_mul_f32 v[238:239], v[238:239], s[64:65]\n\t"
    "v_mfma_f32_16x16x32_bf16 v[120:123], a[184:187], v[8:11], v[120:123]\n\t"
    "v_cvt_pk_bf16_f32 v232, v232, v233\n\t"
    "v_cvt_pk_bf16_f32 v234, v234, v235\n\t"
    "v_cvt_pk_bf16_f32 v236, v236, v237\n\t"
    "v_mfma_f32_16x16x32_bf16 v[124:127], a[188:191], v[8:11], v[124:127]\n\t"
    "v_cvt_pk_bf16_f32 v238, v238, v239\n\t"
    "v_accvgpr_write_b32 a24, v232\n\t"
    "v_accvgpr_write_b32 a25, v234\n\t"
    "v_accvgpr_write_b32 a26, v236\n\t"
    "v_accvgpr_write_b32 a27, v238\n\t"
    "v_lshlrev_b32_e32 v232, 16, v28\n\t"
    "v_and_b32_e32 v233, s66, v28\n\t"
    "v_mfma_f32_16x16x32_bf16 v[16:19], a[104:107], a[24:27], v[16:19]\n\t"
    "v_lshlrev_b32_e32 v234, 16, v29\n\t"
    "v_and_b32_e32 v235, s66, v29\n\t"
    "v_mfma_f32_16x16x32_bf16 v[20:23], a[120:123], a[24:27], v[20:23]\n\t"
    "v_lshlrev_b32_e32 v236, 16, v30\n\t"
    "v_and_b32_e32 v237, s66, v30\n\t"
    "v_lshlrev_b32_e32 v238, 16, v31\n\t"
    "v_and_b32_e32 v239, s66, v31\n\t"
    "v_pk_mul_f32 v[232:233], v[232:233], s[64:65]\n\t"
    "v_pk_mul_f32 v[234:235], v[234:235], s[64:65]\n\t"
    "v_pk_mul_f32 v[236:237], v[236:237], s[64:65]\n\t"
    "v_pk_mul_f32 v[238:239], v[238:239], s[64:65]\n\t"
    "v_smfmac_f32_16x16x64_bf16 v[56:59], v[68:71], v[4:11], v93\n\t"
    "v_cvt_pk_bf16_f32 v232, v232, v233\n\t"
    "v_cvt_pk_bf16_f32 v234, v234, v235\n\t"
    "v_cvt_pk_bf16_f32 v236, v236, v237\n\t"
    "v_cvt_pk_bf16_f32 v238, v238, v239\n\t"
    "v_accvgpr_write_b32 a28, v232\n\t"
    "v_accvgpr_write_b32 a29, v234\n\t"
    "v_accvgpr_write_b32 a30, v236\n\t"
    "v_accvgpr_write_b32 a31, v238\n\t"
    "s_nop 1\n\t"
    "v_mfma_f32_16x16x32_bf16 v[16:19], a[108:111], a[28:31], v[16:19]\n\t"
    "v_mfma_f32_16x16x32_bf16 v[20:23], a[124:127], a[28:31], v[20:23]\n\t"
    "s_add_u32 m0, s57, 0x1c000\n\t"
    "s_add_u32 s67, s71, 0x3000\n\t"
    "buffer_load_dwordx4 v88, s[48:51], s67 offen offset:0 nt lds\n\t"
    "buffer_load_dwordx4 v89, s[48:51], s67 offen offset:1024 nt lds\n\t"
    "buffer_load_dwordx4 v90, s[48:51], s67 offen offset:2048 nt lds\n\t"
    "buffer_load_dwordx4 v91, s[48:51], s67 offen offset:3072 nt lds\n\t"
    "s_add_u32 m0, s57, 0x20000\n\t"
    "s_add_u32 s67, s71, 0x4000\n\t"
    "buffer_load_dwordx4 v88, s[48:51], s67 offen offset:0 nt lds\n\t"
    "buffer_load_dwordx4 v89, s[48:51], s67 offen offset:1024 nt lds\n\t"
    "buffer_load_dwordx4 v90, s[48:51], s67 offen offset:2048 nt lds\n\t"
    "buffer_load_dwordx4 v91, s[48:51], s67 offen offset:3072 nt lds\n\t"
    "v_max3_f32 v240, v16, v17, v18\n\t"
    "v_max3_f32 v240, v240, v19, v20\n\t"
    "v_max3_f32 v240, v240, v21, v22\n\t"
    "v_max_f32_e32 v240, v240, v23\n\t"
    "v_mov_b32_e32 v241, v240\n\t"
    "s_nop 1\n\t"
    "v_permlane32_swap_b32 v240, v241\n\t"
    "v_max_f32_e32 v240, v240, v241\n\t"
    "v_mov_b32_e32 v241, v240\n\t"
    "s_nop 1\n\t"
    "v_permlane16_swap_b32 v240, v241\n\t"
    "v_max_f32_e32 v240, v240, v241\n\t"
    "v_sub_f32_e32 v36, 0, v240\n\t"
    "v_mov_b32_e32 v37, v36\n\t"
    "v_mov_b32_e32 v38, v36\n\t"
    "v_mov_b32_e32 v39, v36\n\t"
    "v_add_f32_e32 v23, v36, v23\n\t"
    "v_add_f32_e32 v22, v36, v22\n\t"
    "v_mfma_f32_16x16x32_bf16 v[24:27], v[192:195], a[16:19], v[36:39]\n\t"
    "v_add_f32_e32 v21, v36, v21\n\t"
    "v_add_f32_e32 v20, v36, v20\n\t"
    "v_mfma_f32_16x16x32_bf16 v[24:27], v[196:199], a[20:23], v[24:27]\n\t"
    "v_add_f32_e32 v19, v36, v19\n\t"
    "v_add_f32_e32 v18, v36, v18\n\t"
    "v_mfma_f32_16x16x32_bf16 v[24:27], v[200:203], a[24:27], v[24:27]\n\t"
    "v_add_f32_e32 v17, v36, v17\n\t"
    "v_add_f32_e32 v16, v36, v16\n\t"
    "v_mfma_f32_16x16x32_bf16 v[24:27], v[204:207], a[28:31], v[24:27]\n\t"
    "v_exp_f32_e32 v23, v23\n\t"
    "v_mfma_f32_16x16x32_bf16 v[28:31], v[208:211], a[16:19], v[36:39]\n\t"
    "v_exp_f32_e32 v22, v22\n\t"
    "v_mfma_f32_16x16x32_bf16 v[28:31], v[212:215], a[20:23], v[28:31]\n\t"
    "v_exp_f32_e32 v21, v21\n\t"
    "v_mfma_f32_16x16x32_bf16 v[28:31], v[216:219], a[24:27], v[28:31]\n\t"
    "v_exp_f32_e32 v20, v20\n\t"
    "v_mfma_f32_16x16x32_bf16 v[28:31], v[220:223], a[28:31], v[28:31]\n\t"
    "v_exp_f32_e32 v19, v19\n\t"
    "v_exp_f32_e32 v18, v18\n\t"
    "v_exp_f32_e32 v17, v17\n\t"
    "v_exp_f32_e32 v16, v16\n\t"
    "s_nop 0\n\t"
    "v_cvt_pk_bf16_f32 v23, v22, v23\n\t"
    "v_cvt_pk_bf16_f32 v22, v20, v21\n\t"
    "v_cvt_pk_bf16_f32 v21, v18, v19\n\t"
    "v_cvt_pk_bf16_f32 v20, v16, v17\n\t"
    "s_waitcnt vmcnt(8)\n\t"
    "ds_read_b128 v[0:3], v224 offset:32768\n\t"
    "ds_read_b128 v[4:7], v225 offset:32768\n\t"
    "ds_read_b128 v[8:11], v226 offset:32768\n\t"
    "ds_read_b128 v[12:15], v227 offset:32768\n\t"
    "v_mfma_f32_16x16x32_bf16 v[128:131], a[128:131], v[20:23], 0\n\t"
    "v_exp_f32_e32 v24, v24\n\t"
    "v_mfma_f32_16x16x32_bf16 v[132:135], a[132:135], v[20:23], 0\n\t"
    "v_exp_f32_e32 v25, v25\n\t"
    "v_mfma_f32_16x16x32_bf16 v[136:139], a[136:139], v[20:23], 0\n\t"
    "v_exp_f32_e32 v26, v26\n\t"
    "v_mfma_f32_16x16x32_bf16 v[140:143], a[140:143], v[20:23], 0\n\t"
    "v_exp_f32_e32 v27, v27\n\t"
    "v_mfma_f32_16x16x32_bf16 v[144:147], a[144:147], v[20:23], 0\n\t"
    "v_exp_f32_e32 v28, v28\n\t"
    "v_mfma_f32_16x16x32_bf16 v[148:151], a[148:151], v[20:23], 0\n\t"
    "v_exp_f32_e32 v29, v29\n\t"
    "v_mfma_f32_16x16x32_bf16 v[152:155], a[152:155], v[20:23], 0\n\t"
    "v_exp_f32_e32 v30, v30\n\t"
    "v_mfma_f32_16x16x32_bf16 v[156:159], a[156:159], v[20:23], 0\n\t"
    "v_exp_f32_e32 v31, v31\n\t"
    "s_waitcnt lgkmcnt(0)\n\t"
    "v_cvt_pk_bf16_f32 v24, v24, v25\n\t"
    "v_cvt_pk_bf16_f32 v25, v26, v27\n\t"
    "v_cvt_pk_bf16_f32 v26, v28, v29\n\t"
    "v_cvt_pk_bf16_f32 v27, v30, v31\n\t"
    "v_lshlrev_b32_e32 v232, 16, v0\n\t"
    "v_and_b32_e32 v233, s66, v0\n\t"
    "v_mfma_f32_16x16x32_bf16 v[128:131], a[160:163], v[24:27], v[128:131]\n\t"
    "v_lshlrev_b32_e32 v234, 16, v1\n\t"
    "v_and_b32_e32 v235, s66, v1\n\t"
    "v_mfma_f32_16x16x32_bf16 v[132:135], a[164:167], v[24:27], v[132:135]\n\t"
    "v_lshlrev_b32_e32 v236, 16, v2\n\t"
    "v_and_b32_e32 v237, s66, v2\n\t"
    "v_lshlrev_b32_e32 v238, 16, v3\n\t"
    "v_and_b32_e32 v239, s66, v3\n\t"
    "v_pk_mul_f32 v[232:233], v[232:233], s[64:65]\n\t"
    "v_pk_mul_f32 v[234:235], v[234:235], s[64:65]\n\t"
    "v_pk_mul_f32 v[236:237], v[236:237], s[64:65]\n\t"
    "v_pk_mul_f32 v[238:239], v[238:239], s[64:65]\n\t"
    "v_mfma_f32_16x16x32_bf16 v[136:139], a[168:171], v[24:27], v[136:139]\n\t"
    "v_cvt_pk_bf16_f32 v232, v232, v233\n\t"
    "v_cvt_pk_bf16_f32 v234, v234, v235\n\t"
    "v_cvt_pk_bf16_f32 v236, v236, v237\n\t"
    "v_mfma_f32_16x16x32_bf16 v[140:143], a[172:175], v[24:27], v[140:143]\n\t"
    "v_cvt_pk_bf16_f32 v238, v238, v239\n\t"
    "v_accvgpr_write_b32 a32, v232\n\t"
    "v_accvgpr_write_b32 a33, v234\n\t"
    "v_accvgpr_write_b32 a34, v236\n\t"
    "v_accvgpr_write_b32 a35, v238\n\t"
    "v_lshlrev_b32_e32 v232, 16, v4\n\t"
    "v_and_b32_e32 v233, s66, v4\n\t"
    "v_mfma_f32_16x16x32_bf16 v[144:147], a[176:179], v[24:27], v[144:147]\n\t"
    "v_lshlrev_b32_e32 v234, 16, v5\n\t"
    "v_and_b32_e32 v235, s66, v5\n\t"
    "v_mfma_f32_16x16x32_bf16 v[148:151], a[180:183], v[24:27], v[148:151]\n\t"
    "v_lshlrev_b32_e32 v236, 16, v6\n\t"
    "v_and_b32_e32 v237, s66, v6\n\t"
    "v_lshlrev_b32_e32 v238, 16, v7\n\t"
    "v_and_b32_e32 v239, s66, v7\n\t"
    "v_pk_mul_f32 v[232:233], v[232:233], s[64:65]\n\t"
    "v_pk_mul_f32 v[234:235], v[234:235], s[64:65]\n\t"
    "v_pk_mul_f32 v[236:237], v[236:237], s[64:65]\n\t"
    "v_pk_mul_f32 v[238:239], v[238:239], s[64:65]\n\t"
    "v_mfma_f32_16x16x32_bf16 v[0:3], a[96:99], a[32:35], 0\n\t"
    "v_cvt_pk_bf16_f32 v232, v232, v233\n\t"
    "v_cvt_pk_bf16_f32 v234, v234, v235\n\t"
    "v_cvt_pk_bf16_f32 v236, v236, v237\n\t"
    "v_mfma_f32_16x16x32_bf16 v[4:7], a[112:115], a[32:35], 0\n\t"
    "v_cvt_pk_bf16_f32 v238, v238, v239\n\t"
    "v_accvgpr_write_b32 a36, v232\n\t"
    "v_accvgpr_write_b32 a37, v234\n\t"
    "v_accvgpr_write_b32 a38, v236\n\t"
    "v_accvgpr_write_b32 a39, v238\n\t"
    "v_lshlrev_b32_e32 v232, 16, v8\n\t"
    "v_and_b32_e32 v233, s66, v8\n\t"
    "v_mfma_f32_16x16x32_bf16 v[0:3], a[100:103], a[36:39], v[0:3]\n\t"
    "v_lshlrev_b32_e32 v234, 16, v9\n\t"
    "v_and_b32_e32 v235, s66, v9\n\t"
    "v_mfma_f32_16x16x32_bf16 v[4:7], a[116:119], a[36:39], v[4:7]\n\t"
    "v_lshlrev_b32_e32 v236, 16, v10\n\t"
    "v_and_b32_e32 v237, s66, v10\n\t"
    "v_lshlrev_b32_e32 v238, 16, v11\n\t"
    "v_and_b32_e32 v239, s66, v11\n\t"
    "v_pk_mul_f32 v[232:233], v[232:233], s[64:65]\n\t"
    "v_pk_mul_f32 v[234:235], v[234:235], s[64:65]\n\t"
    "v_pk_mul_f32 v[236:237], v[236:237], s[64:65]\n\t"
    "v_pk_mul_f32 v[238:239], v[238:239], s[64:65]\n\t"
    "v_mfma_f32_16x16x32_bf16 v[152:155], a[184:187], v[24:27], v[152:155]\n\t"
    "v_cvt_pk_bf16_f32 v232, v232, v233\n\t"
    "v_cvt_pk_bf16_f32 v234, v234, v235\n\t"
    "v_cvt_pk_bf16_f32 v236, v236, v237\n\t"
    "v_mfma_f32_16x16x32_bf16 v[156:159], a[188:191], v[24:27], v[156:159]\n\t"
    "v_cvt_pk_bf16_f32 v238, v238, v239\n\t"
    "v_accvgpr_write_b32 a40, v232\n\t"
    "v_accvgpr_write_b32 a41, v234\n\t"
    "v_accvgpr_write_b32 a42, v236\n\t"
    "v_accvgpr_write_b32 a43, v238\n\t"
    "v_lshlrev_b32_e32 v232, 16, v12\n\t"
    "v_and_b32_e32 v233, s66, v12\n\t"
    "v_mfma_f32_16x16x32_bf16 v[0:3], a[104:107], a[40:43], v[0:3]\n\t"
    "v_lshlrev_b32_e32 v234, 16, v13\n\t"
    "v_and_b32_e32 v235, s66, v13\n\t"
    "v_mfma_f32_16x16x32_bf16 v[4:7], a[120:123], a[40:43], v[4:7]\n\t"
    "v_lshlrev_b32_e32 v236, 16, v14\n\t"
    "v_and_b32_e32 v237, s66, v14\n\t"
    "v_lshlrev_b32_e32 v238, 16, v15\n\t"
    "v_and_b32_e32 v239, s66, v15\n\t"
    "v_pk_mul_f32 v[232:233], v[232:233], s[64:65]\n\t"
    "v_pk_mul_f32 v[234:235], v[234:235], s[64:65]\n\t"
    "v_pk_mul_f32 v[236:237], v[236:237], s[64:65]\n\t"
    "v_pk_mul_f32 v[238:239], v[238:239], s[64:65]\n\t"
    "v_smfmac_f32_16x16x64_bf16 v[56:59], v[252:255], v[20:27], v93\n\t"
    "v_cvt_pk_bf16_f32 v232, v232, v233\n\t"
    "v_cvt_pk_bf16_f32 v234, v234, v235\n\t"
    "v_cvt_pk_bf16_f32 v236, v236, v237\n\t"
    "v_cvt_pk_bf16_f32 v238, v238, v239\n\t"
    "v_accvgpr_write_b32 a44, v232\n\t"
    "v_accvgpr_write_b32 a45, v234\n\t"
    "v_accvgpr_write_b32 a46, v236\n\t"
    "v_accvgpr_write_b32 a47, v238\n\t"
    "s_nop 1\n\t"
    "v_mfma_f32_16x16x32_bf16 v[0:3], a[108:111], a[44:47], v[0:3]\n\t"
    "v_mfma_f32_16x16x32_bf16 v[4:7], a[124:127], a[44:47], v[4:7]\n\t"
    "s_add_u32 m0, s57, 0x24000\n\t"
    "s_add_u32 s67, s71, 0x5000\n\t"
    "s_cmp_eq_u32 %[wid], 3\n\t"
    "s_cselect_b32 s50, 0, s50\n\t"
    "buffer_load_dwordx4 v88, s[48:51], s67 offen offset:0 nt lds\n\t"
    "buffer_load_dwordx4 v89, s[48:51], s67 offen offset:1024 nt lds\n\t"
    "buffer_load_dwordx4 v90, s[48:51], s67 offen offset:2048 nt lds\n\t"
    "buffer_load_dwordx4 v91, s[48:51], s67 offen offset:3072 nt lds\n\t"
    "v_max3_f32 v240, v0, v1, v2\n\t"
    "v_max3_f32 v240, v240, v3, v4\n\t"
    "v_max3_f32 v240, v240, v5, v6\n\t"
    "v_max_f32_e32 v240, v240, v7\n\t"
    "v_mov_b32_e32 v241, v240\n\t"
    "s_nop 1\n\t"
    "v_permlane32_swap_b32 v240, v241\n\t"
    "v_max_f32_e32 v240, v240, v241\n\t"
    "v_mov_b32_e32 v241, v240\n\t"
    "s_nop 1\n\t"
    "v_permlane16_swap_b32 v240, v241\n\t"
    "v_max_f32_e32 v240, v240, v241\n\t"
    "v_sub_f32_e32 v40, 0, v240\n\t"
    "v_mov_b32_e32 v41, v40\n\t"
    "v_mov_b32_e32 v42, v40\n\t"
    "v_mov_b32_e32 v43, v40\n\t"
    "v_add_f32_e32 v7, v40, v7\n\t"
    "v_add_f32_e32 v6, v40, v6\n\t"
    "v_mfma_f32_16x16x32_bf16 v[8:11], v[192:195], a[32:35], v[40:43]\n\t"
    "v_add_f32_e32 v5, v40, v5\n\t"
    "v_add_f32_e32 v4, v40, v4\n\t"
    "v_mfma_f32_16x16x32_bf16 v[8:11], v[196:199], a[36:39], v[8:11]\n\t"
    "v_add_f32_e32 v3, v40, v3\n\t"
    "v_add_f32_e32 v2, v40, v2\n\t"
    "v_mfma_f32_16x16x32_bf16 v[8:11], v[200:203], a[40:43], v[8:11]\n\t"
    "v_add_f32_e32 v1, v40, v1\n\t"
    "v_add_f32_e32 v0, v40, v0\n\t"
    "v_mfma_f32_16x16x32_bf16 v[8:11], v[204:207], a[44:47], v[8:11]\n\t"
    "v_exp_f32_e32 v7, v7\n\t"
    "v_mfma_f32_16x16x32_bf16 v[12:15], v[208:211], a[32:35], v[40:43]\n\t"
    "v_exp_f32_e32 v6, v6\n\t"
    "v_mfma_f32_16x16x32_bf16 v[12:15], v[212:215], a[36:39], v[12:15]\n\t"
    "v_exp_f32_e32 v5, v5\n\t"
    "v_mfma_f32_16x16x32_bf16 v[12:15], v[216:219], a[40:43], v[12:15]\n\t"
    "v_exp_f32_e32 v4, v4\n\t"
    "v_mfma_f32_16x16x32_bf16 v[12:15], v[220:223], a[44:47], v[12:15]\n\t"
    "v_exp_f32_e32 v3, v3\n\t"
    "v_exp_f32_e32 v2, v2\n\t"
    "v_exp_f32_e32 v1, v1\n\t"
    "v_exp_f32_e32 v0, v0\n\t"
    "s_nop 0\n\t"
    "v_cvt_pk_bf16_f32 v7, v6, v7\n\t"
    "v_cvt_pk_bf16_f32 v6, v4, v5\n\t"
    "v_cvt_pk_bf16_f32 v5, v2, v3\n\t"
    "v_cvt_pk_bf16_f32 v4, v0, v1\n\t"
    "s_waitcnt vmcnt(8)\n\t"
    "ds_read_b128 v[16:19], v224 offset:49152\n\t"
    "ds_read_b128 v[20:23], v225 offset:49152\n\t"
    "ds_read_b128 v[24:27], v226 offset:49152\n\t"
    "ds_read_b128 v[28:31], v227 offset:49152\n\t"
    "v_mfma_f32_16x16x32_bf16 v[160:163], a[128:131], v[4:7], 0\n\t"
    "v_exp_f32_e32 v8, v8\n\t"
    "v_mfma_f32_16x16x32_bf16 v[164:167], a[132:135], v[4:7], 0\n\t"
    "v_exp_f32_e32 v9, v9\n\t"
    "v_mfma_f32_16x16x32_bf16 v[168:171], a[136:139], v[4:7], 0\n\t"
    "v_exp_f32_e32 v10, v10\n\t"
    "v_mfma_f32_16x16x32_bf16 v[172:175], a[140:143], v[4:7], 0\n\t"
    "v_exp_f32_e32 v11, v11\n\t"
    "v_mfma_f32_16x16x32_bf16 v[176:179], a[144:147], v[4:7], 0\n\t"
    "v_exp_f32_e32 v12, v12\n\t"
    "v_mfma_f32_16x16x32_bf16 v[180:183], a[148:151], v[4:7], 0\n\t"
    "v_exp_f32_e32 v13, v13\n\t"
    "v_mfma_f32_16x16x32_bf16 v[184:187], a[152:155], v[4:7], 0\n\t"
    "v_exp_f32_e32 v14, v14\n\t"
    "v_mfma_f32_16x16x32_bf16 v[188:191], a[156:159], v[4:7], 0\n\t"
    "v_exp_f32_e32 v15, v15\n\t"
    "s_waitcnt lgkmcnt(0)\n\t"
    "v_cvt_pk_bf16_f32 v8, v8, v9\n\t"
    "v_cvt_pk_bf16_f32 v9, v10, v11\n\t"
    "v_cvt_pk_bf16_f32 v10, v12, v13\n\t"
    "v_cvt_pk_bf16_f32 v11, v14, v15\n\t"
    "v_lshlrev_b32_e32 v232, 16, v16\n\t"
    "v_and_b32_e32 v233, s66, v16\n\t"
    "v_mfma_f32_16x16x32_bf16 v[160:163], a[160:163], v[8:11], v[160:163]\n\t"
    "v_lshlrev_b32_e32 v234, 16, v17\n\t"
    "v_and_b32_e32 v235, s66, v17\n\t"
    "v_mfma_f32_16x16x32_bf16 v[164:167], a[164:167], v[8:11], v[164:167]\n\t"
    "v_lshlrev_b32_e32 v236, 16, v18\n\t"
    "v_and_b32_e32 v237, s66, v18\n\t"
    "v_lshlrev_b32_e32 v238, 16, v19\n\t"
    "v_and_b32_e32 v239, s66, v19\n\t"
    "v_pk_mul_f32 v[232:233], v[232:233], s[64:65]\n\t"
    "v_pk_mul_f32 v[234:235], v[234:235], s[64:65]\n\t"
    "v_pk_mul_f32 v[236:237], v[236:237], s[64:65]\n\t"
    "v_pk_mul_f32 v[238:239], v[238:239], s[64:65]\n\t"
    "v_mfma_f32_16x16x32_bf16 v[168:171], a[168:171], v[8:11], v[168:171]\n\t"
    "v_cvt_pk_bf16_f32 v232, v232, v233\n\t"
    "v_cvt_pk_bf16_f32 v234, v234, v235\n\t"
    "v_cvt_pk_bf16_f32 v236, v236, v237\n\t"
    "v_mfma_f32_16x16x32_bf16 v[172:175], a[172:175], v[8:11], v[172:175]\n\t"
    "v_cvt_pk_bf16_f32 v238, v238, v239\n\t"
    "v_accvgpr_write_b32 a48, v232\n\t"
    "v_accvgpr_write_b32 a49, v234\n\t"
    "v_accvgpr_write_b32 a50, v236\n\t"
    "v_accvgpr_write_b32 a51, v238\n\t"
    "v_lshlrev_b32_e32 v232, 16, v20\n\t"
    "v_and_b32_e32 v233, s66, v20\n\t"
    "v_mfma_f32_16x16x32_bf16 v[176:179], a[176:179], v[8:11], v[176:179]\n\t"
    "v_lshlrev_b32_e32 v234, 16, v21\n\t"
    "v_and_b32_e32 v235, s66, v21\n\t"
    "v_mfma_f32_16x16x32_bf16 v[180:183], a[180:183], v[8:11], v[180:183]\n\t"
    "v_lshlrev_b32_e32 v236, 16, v22\n\t"
    "v_and_b32_e32 v237, s66, v22\n\t"
    "v_lshlrev_b32_e32 v238, 16, v23\n\t"
    "v_and_b32_e32 v239, s66, v23\n\t"
    "v_pk_mul_f32 v[232:233], v[232:233], s[64:65]\n\t"
    "v_pk_mul_f32 v[234:235], v[234:235], s[64:65]\n\t"
    "v_pk_mul_f32 v[236:237], v[236:237], s[64:65]\n\t"
    "v_pk_mul_f32 v[238:239], v[238:239], s[64:65]\n\t"
    "v_mfma_f32_16x16x32_bf16 v[16:19], a[96:99], a[48:51], 0\n\t"
    "v_cvt_pk_bf16_f32 v232, v232, v233\n\t"
    "v_cvt_pk_bf16_f32 v234, v234, v235\n\t"
    "v_cvt_pk_bf16_f32 v236, v236, v237\n\t"
    "v_mfma_f32_16x16x32_bf16 v[20:23], a[112:115], a[48:51], 0\n\t"
    "v_cvt_pk_bf16_f32 v238, v238, v239\n\t"
    "v_accvgpr_write_b32 a52, v232\n\t"
    "v_accvgpr_write_b32 a53, v234\n\t"
    "v_accvgpr_write_b32 a54, v236\n\t"
    "v_accvgpr_write_b32 a55, v238\n\t"
    "v_lshlrev_b32_e32 v232, 16, v24\n\t"
    "v_and_b32_e32 v233, s66, v24\n\t"
    "v_mfma_f32_16x16x32_bf16 v[16:19], a[100:103], a[52:55], v[16:19]\n\t"
    "v_lshlrev_b32_e32 v234, 16, v25\n\t"
    "v_and_b32_e32 v235, s66, v25\n\t"
    "v_mfma_f32_16x16x32_bf16 v[20:23], a[116:119], a[52:55], v[20:23]\n\t"
    "v_lshlrev_b32_e32 v236, 16, v26\n\t"
    "v_and_b32_e32 v237, s66, v26\n\t"
    "v_lshlrev_b32_e32 v238, 16, v27\n\t"
    "v_and_b32_e32 v239, s66, v27\n\t"
    "v_pk_mul_f32 v[232:233], v[232:233], s[64:65]\n\t"
    "v_pk_mul_f32 v[234:235], v[234:235], s[64:65]\n\t"
    "v_pk_mul_f32 v[236:237], v[236:237], s[64:65]\n\t"
    "v_pk_mul_f32 v[238:239], v[238:239], s[64:65]\n\t"
    "v_mfma_f32_16x16x32_bf16 v[184:187], a[184:187], v[8:11], v[184:187]\n\t"
    "v_cvt_pk_bf16_f32 v232, v232, v233\n\t"
    "v_cvt_pk_bf16_f32 v234, v234, v235\n\t"
    "v_cvt_pk_bf16_f32 v236, v236, v237\n\t"
    "v_mfma_f32_16x16x32_bf16 v[188:191], a[188:191], v[8:11], v[188:191]\n\t"
    "v_cvt_pk_bf16_f32 v238, v238, v239\n\t"
    "v_accvgpr_write_b32 a56, v232\n\t"
    "v_accvgpr_write_b32 a57, v234\n\t"
    "v_accvgpr_write_b32 a58, v236\n\t"
    "v_accvgpr_write_b32 a59, v238\n\t"
    "v_lshlrev_b32_e32 v232, 16, v28\n\t"
    "v_and_b32_e32 v233, s66, v28\n\t"
    "v_mfma_f32_16x16x32_bf16 v[16:19], a[104:107], a[56:59], v[16:19]\n\t"
    "v_lshlrev_b32_e32 v234, 16, v29\n\t"
    "v_and_b32_e32 v235, s66, v29\n\t"
    "v_mfma_f32_16x16x32_bf16 v[20:23], a[120:123], a[56:59], v[20:23]\n\t"
    "v_lshlrev_b32_e32 v236, 16, v30\n\t"
    "v_and_b32_e32 v237, s66, v30\n\t"
    "v_lshlrev_b32_e32 v238, 16, v31\n\t"
    "v_and_b32_e32 v239, s66, v31\n\t"
    "v_pk_mul_f32 v[232:233], v[232:233], s[64:65]\n\t"
    "v_pk_mul_f32 v[234:235], v[234:235], s[64:65]\n\t"
    "v_pk_mul_f32 v[236:237], v[236:237], s[64:65]\n\t"
    "v_pk_mul_f32 v[238:239], v[238:239], s[64:65]\n\t"
    "v_smfmac_f32_16x16x64_bf16 v[60:63], v[68:71], v[4:11], v93\n\t"
    "v_cvt_pk_bf16_f32 v232, v232, v233\n\t"
    "v_cvt_pk_bf16_f32 v234, v234, v235\n\t"
    "v_cvt_pk_bf16_f32 v236, v236, v237\n\t"
    "v_cvt_pk_bf16_f32 v238, v238, v239\n\t"
    "v_accvgpr_write_b32 a60, v232\n\t"
    "v_accvgpr_write_b32 a61, v234\n\t"
    "v_accvgpr_write_b32 a62, v236\n\t"
    "v_accvgpr_write_b32 a63, v238\n\t"
    "s_nop 1\n\t"
    "v_mfma_f32_16x16x32_bf16 v[16:19], a[108:111], a[60:63], v[16:19]\n\t"
    "v_mfma_f32_16x16x32_bf16 v[20:23], a[124:127], a[60:63], v[20:23]\n\t"
    "s_add_u32 m0, s75, 0x8000\n\t"
    "s_nop 0\n\t"
    "buffer_load_dwordx4 v245, s[40:43], s56 offen offset:0 nt lds\n\t"
    "buffer_load_dwordx4 v246, s[40:43], s56 offen offset:1024 nt lds\n\t"
    "s_add_u32 m0, s75, 0xc000\n\t"
    "s_nop 0\n\t"
    "buffer_load_dwordx4 v92, s[44:47], s56 offen offset:0 nt lds\n\t"
    "buffer_load_dwordx4 v92, s[44:47], s56 offen offset:1024 nt lds\n\t"
    "s_add_u32 s56, s56, 0x2000\n\t"
    "s_and_b32 s56, s56, 0x3ffff\n\t"
    "v_max3_f32 v240, v16, v17, v18\n\t"
    "v_max3_f32 v240, v240, v19, v20\n\t"
    "v_max3_f32 v240, v240, v21, v22\n\t"
    "v_max_f32_e32 v240, v240, v23\n\t"
    "v_mov_b32_e32 v241, v240\n\t"
    "s_nop 1\n\t"
    "v_permlane32_swap_b32 v240, v241\n\t"
    "v_max_f32_e32 v240, v240, v241\n\t"
    "v_mov_b32_e32 v241, v240\n\t"
    "s_nop 1\n\t"
    "v_permlane16_swap_b32 v240, v241\n\t"
    "v_max_f32_e32 v240, v240, v241\n\t"
    "v_sub_f32_e32 v44, 0, v240\n\t"
    "v_mov_b32_e32 v45, v44\n\t"
    "v_mov_b32_e32 v46, v44\n\t"
    "v_mov_b32_e32 v47, v44\n\t"
    "v_add_f32_e32 v23, v44, v23\n\t"
    "v_add_f32_e32 v22, v44, v22\n\t"
    "v_mfma_f32_16x16x32_bf16 v[24:27], v[192:195], a[48:51], v[44:47]\n\t"
    "v_add_f32_e32 v21, v44, v21\n\t"
    "v_add_f32_e32 v20, v44, v20\n\t"
    "v_mfma_f32_16x16x32_bf16 v[24:27], v[196:199], a[52:55], v[24:27]\n\t"
    "v_add_f32_e32 v19, v44, v19\n\t"
    "v_add_f32_e32 v18, v44, v18\n\t"
    "v_mfma_f32_16x16x32_bf16 v[24:27], v[200:203], a[56:59], v[24:27]\n\t"
    "v_add_f32_e32 v17, v44, v17\n\t"
    "v_add_f32_e32 v16, v44, v16\n\t"
    "v_mfma_f32_16x16x32_bf16 v[24:27], v[204:207], a[60:63], v[24:27]\n\t"
    "v_exp_f32_e32 v23, v23\n\t"
    "v_mfma_f32_16x16x32_bf16 v[28:31], v[208:211], a[48:51], v[44:47]\n\t"
    "v_exp_f32_e32 v22, v22\n\t"
    "v_mfma_f32_16x16x32_bf16 v[28:31], v[212:215], a[52:55], v[28:31]\n\t"
    "v_exp_f32_e32 v21, v21\n\t"
    "v_mfma_f32_16x16x32_bf16 v[28:31], v[216:219], a[56:59], v[28:31]\n\t"
    "v_exp_f32_e32 v20, v20\n\t"
    "v_mfma_f32_16x16x32_bf16 v[28:31], v[220:223], a[60:63], v[28:31]\n\t"
    "v_exp_f32_e32 v19, v19\n\t"
    "v_exp_f32_e32 v18, v18\n\t"
    "v_exp_f32_e32 v17, v17\n\t"
    "v_exp_f32_e32 v16, v16\n\t"
    "s_nop 0\n\t"
    "v_cvt_pk_bf16_f32 v23, v22, v23\n\t"
    "v_cvt_pk_bf16_f32 v22, v20, v21\n\t"
    "v_cvt_pk_bf16_f32 v21, v18, v19\n\t"
    "v_cvt_pk_bf16_f32 v20, v16, v17\n\t"
    "s_waitcnt vmcnt(8)\n\t"
    "ds_read_b128 v[0:3], v228 offset:0\n\t"
    "ds_read_b128 v[4:7], v229 offset:0\n\t"
    "ds_read_b128 v[8:11], v230 offset:0\n\t"
    "ds_read_b128 v[12:15], v231 offset:0\n\t"
    "v_mfma_f32_16x16x32_bf16 a[192:195], a[128:131], v[20:23], 0\n\t"
    "v_exp_f32_e32 v24, v24\n\t"
    "v_mfma_f32_16x16x32_bf16 a[196:199], a[132:135], v[20:23], 0\n\t"
    "v_exp_f32_e32 v25, v25\n\t"
    "v_mfma_f32_16x16x32_bf16 a[200:203], a[136:139], v[20:23], 0\n\t"
    "v_exp_f32_e32 v26, v26\n\t"
    "v_mfma_f32_16x16x32_bf16 a[204:207], a[140:143], v[20:23], 0\n\t"
    "v_exp_f32_e32 v27, v27\n\t"
    "v_mfma_f32_16x16x32_bf16 a[208:211], a[144:147], v[20:23], 0\n\t"
    "v_exp_f32_e32 v28, v28\n\t"
    "v_mfma_f32_16x16x32_bf16 a[212:215], a[148:151], v[20:23], 0\n\t"
    "v_exp_f32_e32 v29, v29\n\t"
    "v_mfma_f32_16x16x32_bf16 a[216:219], a[152:155], v[20:23], 0\n\t"
    "v_exp_f32_e32 v30, v30\n\t"
    "v_mfma_f32_16x16x32_bf16 a[220:223], a[156:159], v[20:23], 0\n\t"
    "v_exp_f32_e32 v31, v31\n\t"
    "s_waitcnt lgkmcnt(0)\n\t"
    "v_cvt_pk_bf16_f32 v24, v24, v25\n\t"
    "v_cvt_pk_bf16_f32 v25, v26, v27\n\t"
    "v_cvt_pk_bf16_f32 v26, v28, v29\n\t"
    "v_cvt_pk_bf16_f32 v27, v30, v31\n\t"
    "v_lshlrev_b32_e32 v232, 16, v0\n\t"
    "v_and_b32_e32 v233, s66, v0\n\t"
    "v_mfma_f32_16x16x32_bf16 a[192:195], a[160:163], v[24:27], a[192:195]\n\t"
    "v_lshlrev_b32_e32 v234, 16, v1\n\t"
    "v_and_b32_e32 v235, s66, v1\n\t"
    "v_mfma_f32_16x16x32_bf16 a[196:199], a[164:167], v[24:27], a[196:199]\n\t"
    "v_lshlrev_b32_e32 v236, 16, v2\n\t"
    "v_and_b32_e32 v237, s66, v2\n\t"
    "v_lshlrev_b32_e32 v238, 16, v3\n\t"
    "v_and_b32_e32 v239, s66, v3\n\t"
    "v_pk_mul_f32 v[232:233], v[232:233], s[64:65]\n\t"
    "v_pk_mul_f32 v[234:235], v[234:235], s[64:65]\n\t"
    "v_pk_mul_f32 v[236:237], v[236:237], s[64:65]\n\t"
    "v_pk_mul_f32 v[238:239], v[238:239], s[64:65]\n\t"
    "v_mfma_f32_16x16x32_bf16 a[200:203], a[168:171], v[24:27], a[200:203]\n\t"
    "v_cvt_pk_bf16_f32 v232, v232, v233\n\t"
    "v_cvt_pk_bf16_f32 v234, v234, v235\n\t"
    "v_cvt_pk_bf16_f32 v236, v236, v237\n\t"
    "v_mfma_f32_16x16x32_bf16 a[204:207], a[172:175], v[24:27], a[204:207]\n\t"
    "v_cvt_pk_bf16_f32 v238, v238, v239\n\t"
    "v_accvgpr_write_b32 a64, v232\n\t"
    "v_accvgpr_write_b32 a65, v234\n\t"
    "v_accvgpr_write_b32 a66, v236\n\t"
    "v_accvgpr_write_b32 a67, v238\n\t"
    "v_lshlrev_b32_e32 v232, 16, v4\n\t"
    "v_and_b32_e32 v233, s66, v4\n\t"
    "v_mfma_f32_16x16x32_bf16 a[208:211], a[176:179], v[24:27], a[208:211]\n\t"
    "v_lshlrev_b32_e32 v234, 16, v5\n\t"
    "v_and_b32_e32 v235, s66, v5\n\t"
    "v_mfma_f32_16x16x32_bf16 a[212:215], a[180:183], v[24:27], a[212:215]\n\t"
    "v_lshlrev_b32_e32 v236, 16, v6\n\t"
    "v_and_b32_e32 v237, s66, v6\n\t"
    "v_lshlrev_b32_e32 v238, 16, v7\n\t"
    "v_and_b32_e32 v239, s66, v7\n\t"
    "v_pk_mul_f32 v[232:233], v[232:233], s[64:65]\n\t"
    "v_pk_mul_f32 v[234:235], v[234:235], s[64:65]\n\t"
    "v_pk_mul_f32 v[236:237], v[236:237], s[64:65]\n\t"
    "v_pk_mul_f32 v[238:239], v[238:239], s[64:65]\n\t"
    "v_mfma_f32_16x16x32_bf16 v[0:3], a[96:99], a[64:67], 0\n\t"
    "v_cvt_pk_bf16_f32 v232, v232, v233\n\t"
    "v_cvt_pk_bf16_f32 v234, v234, v235\n\t"
    "v_cvt_pk_bf16_f32 v236, v236, v237\n\t"
    "v_mfma_f32_16x16x32_bf16 v[4:7], a[112:115], a[64:67], 0\n\t"
    "v_cvt_pk_bf16_f32 v238, v238, v239\n\t"
    "v_accvgpr_write_b32 a68, v232\n\t"
    "v_accvgpr_write_b32 a69, v234\n\t"
    "v_accvgpr_write_b32 a70, v236\n\t"
    "v_accvgpr_write_b32 a71, v238\n\t"
    "v_lshlrev_b32_e32 v232, 16, v8\n\t"
    "v_and_b32_e32 v233, s66, v8\n\t"
    "v_mfma_f32_16x16x32_bf16 v[0:3], a[100:103], a[68:71], v[0:3]\n\t"
    "v_lshlrev_b32_e32 v234, 16, v9\n\t"
    "v_and_b32_e32 v235, s66, v9\n\t"
    "v_mfma_f32_16x16x32_bf16 v[4:7], a[116:119], a[68:71], v[4:7]\n\t"
    "v_lshlrev_b32_e32 v236, 16, v10\n\t"
    "v_and_b32_e32 v237, s66, v10\n\t"
    "v_lshlrev_b32_e32 v238, 16, v11\n\t"
    "v_and_b32_e32 v239, s66, v11\n\t"
    "v_pk_mul_f32 v[232:233], v[232:233], s[64:65]\n\t"
    "v_pk_mul_f32 v[234:235], v[234:235], s[64:65]\n\t"
    "v_pk_mul_f32 v[236:237], v[236:237], s[64:65]\n\t"
    "v_pk_mul_f32 v[238:239], v[238:239], s[64:65]\n\t"
    "v_mfma_f32_16x16x32_bf16 a[216:219], a[184:187], v[24:27], a[216:219]\n\t"
    "v_cvt_pk_bf16_f32 v232, v232, v233\n\t"
    "v_cvt_pk_bf16_f32 v234, v234, v235\n\t"
    "v_cvt_pk_bf16_f32 v236, v236, v237\n\t"
    "v_mfma_f32_16x16x32_bf16 a[220:223], a[188:191], v[24:27], a[220:223]\n\t"
    "v_cvt_pk_bf16_f32 v238, v238, v239\n\t"
    "v_accvgpr_write_b32 a72, v232\n\t"
    "v_accvgpr_write_b32 a73, v234\n\t"
    "v_accvgpr_write_b32 a74, v236\n\t"
    "v_accvgpr_write_b32 a75, v238\n\t"
    "v_lshlrev_b32_e32 v232, 16, v12\n\t"
    "v_and_b32_e32 v233, s66, v12\n\t"
    "v_mfma_f32_16x16x32_bf16 v[0:3], a[104:107], a[72:75], v[0:3]\n\t"
    "v_lshlrev_b32_e32 v234, 16, v13\n\t"
    "v_and_b32_e32 v235, s66, v13\n\t"
    "v_mfma_f32_16x16x32_bf16 v[4:7], a[120:123], a[72:75], v[4:7]\n\t"
    "v_lshlrev_b32_e32 v236, 16, v14\n\t"
    "v_and_b32_e32 v237, s66, v14\n\t"
    "v_lshlrev_b32_e32 v238, 16, v15\n\t"
    "v_and_b32_e32 v239, s66, v15\n\t"
    "v_pk_mul_f32 v[232:233], v[232:233], s[64:65]\n\t"
    "v_pk_mul_f32 v[234:235], v[234:235], s[64:65]\n\t"
    "v_pk_mul_f32 v[236:237], v[236:237], s[64:65]\n\t"
    "v_pk_mul_f32 v[238:239], v[238:239], s[64:65]\n\t"
    "v_smfmac_f32_16x16x64_bf16 v[60:63], v[252:255], v[20:27], v93\n\t"
    "v_cvt_pk_bf16_f32 v232, v232, v233\n\t"
    "v_cvt_pk_bf16_f32 v234, v234, v235\n\t"
    "v_cvt_pk_bf16_f32 v236, v236, v237\n\t"
    "v_cvt_pk_bf16_f32 v238, v238, v239\n\t"
    "v_accvgpr_write_b32 a76, v232\n\t"
    "v_accvgpr_write_b32 a77, v234\n\t"
    "v_accvgpr_write_b32 a78, v236\n\t"
    "v_accvgpr_write_b32 a79, v238\n\t"
    "s_nop 1\n\t"
    "v_mfma_f32_16x16x32_bf16 v[0:3], a[108:111], a[76:79], v[0:3]\n\t"
    "v_mfma_f32_16x16x32_bf16 v[4:7], a[124:127], a[76:79], v[4:7]\n\t"
    "s_add_u32 m0, s75, 0xa000\n\t"
    "s_nop 0\n\t"
    "buffer_load_dwordx4 v245, s[40:43], s56 offen offset:0 nt lds\n\t"
    "buffer_load_dwordx4 v246, s[40:43], s56 offen offset:1024 nt lds\n\t"
    "s_add_u32 m0, s75, 0xe000\n\t"
    "s_nop 0\n\t"
    "buffer_load_dwordx4 v92, s[44:47], s56 offen offset:0 nt lds\n\t"
    "buffer_load_dwordx4 v92, s[44:47], s56 offen offset:1024 nt lds\n\t"
    "s_add_u32 s56, s56, 0x2000\n\t"
    "s_and_b32 s56, s56, 0x3ffff\n\t"
    "v_max3_f32 v240, v0, v1, v2\n\t"
    "v_max3_f32 v240, v240, v3, v4\n\t"
    "v_max3_f32 v240, v240, v5, v6\n\t"
    "v_max_f32_e32 v240, v240, v7\n\t"
    "v_mov_b32_e32 v241, v240\n\t"
    "s_nop 1\n\t"
    "v_permlane32_swap_b32 v240, v241\n\t"
    "v_max_f32_e32 v240, v240, v241\n\t"
    "v_mov_b32_e32 v241, v240\n\t"
    "s_nop 1\n\t"
    "v_permlane16_swap_b32 v240, v241\n\t"
    "v_max_f32_e32 v240, v240, v241\n\t"
    "v_sub_f32_e32 v48, 0, v240\n\t"
    "v_mov_b32_e32 v49, v48\n\t"
    "v_mov_b32_e32 v50, v48\n\t"
    "v_mov_b32_e32 v51, v48\n\t"
    "v_add_f32_e32 v7, v48, v7\n\t"
    "v_add_f32_e32 v6, v48, v6\n\t"
    "v_mfma_f32_16x16x32_bf16 v[8:11], v[192:195], a[64:67], v[48:51]\n\t"
    "v_add_f32_e32 v5, v48, v5\n\t"
    "v_add_f32_e32 v4, v48, v4\n\t"
    "v_mfma_f32_16x16x32_bf16 v[8:11], v[196:199], a[68:71], v[8:11]\n\t"
    "v_add_f32_e32 v3, v48, v3\n\t"
    "v_add_f32_e32 v2, v48, v2\n\t"
    "v_mfma_f32_16x16x32_bf16 v[8:11], v[200:203], a[72:75], v[8:11]\n\t"
    "v_add_f32_e32 v1, v48, v1\n\t"
    "v_add_f32_e32 v0, v48, v0\n\t"
    "v_mfma_f32_16x16x32_bf16 v[8:11], v[204:207], a[76:79], v[8:11]\n\t"
    "v_exp_f32_e32 v7, v7\n\t"
    "v_mfma_f32_16x16x32_bf16 v[12:15], v[208:211], a[64:67], v[48:51]\n\t"
    "v_exp_f32_e32 v6, v6\n\t"
    "v_mfma_f32_16x16x32_bf16 v[12:15], v[212:215], a[68:71], v[12:15]\n\t"
    "v_exp_f32_e32 v5, v5\n\t"
    "v_mfma_f32_16x16x32_bf16 v[12:15], v[216:219], a[72:75], v[12:15]\n\t"
    "v_exp_f32_e32 v4, v4\n\t"
    "v_mfma_f32_16x16x32_bf16 v[12:15], v[220:223], a[76:79], v[12:15]\n\t"
    "v_exp_f32_e32 v3, v3\n\t"
    "v_exp_f32_e32 v2, v2\n\t"
    "v_exp_f32_e32 v1, v1\n\t"
    "v_exp_f32_e32 v0, v0\n\t"
    "s_nop 0\n\t"
    "v_cvt_pk_bf16_f32 v7, v6, v7\n\t"
    "v_cvt_pk_bf16_f32 v6, v4, v5\n\t"
    "v_cvt_pk_bf16_f32 v5, v2, v3\n\t"
    "v_cvt_pk_bf16_f32 v4, v0, v1\n\t"
    "s_waitcnt vmcnt(8)\n\t"
    "ds_read_b128 v[16:19], v228 offset:16384\n\t"
    "ds_read_b128 v[20:23], v229 offset:16384\n\t"
    "ds_read_b128 v[24:27], v230 offset:16384\n\t"
    "ds_read_b128 v[28:31], v231 offset:16384\n\t"
    "v_mfma_f32_16x16x32_bf16 a[224:227], a[128:131], v[4:7], 0\n\t"
    "v_exp_f32_e32 v8, v8\n\t"
    "v_mfma_f32_16x16x32_bf16 a[228:231], a[132:135], v[4:7], 0\n\t"
    "v_exp_f32_e32 v9, v9\n\t"
    "v_mfma_f32_16x16x32_bf16 a[232:235], a[136:139], v[4:7], 0\n\t"
    "v_exp_f32_e32 v10, v10\n\t"
    "v_mfma_f32_16x16x32_bf16 a[236:239], a[140:143], v[4:7], 0\n\t"
    "v_exp_f32_e32 v11, v11\n\t"
    "v_mfma_f32_16x16x32_bf16 a[240:243], a[144:147], v[4:7], 0\n\t"
    "v_exp_f32_e32 v12, v12\n\t"
    "v_mfma_f32_16x16x32_bf16 a[244:247], a[148:151], v[4:7], 0\n\t"
    "v_exp_f32_e32 v13, v13\n\t"
    "v_mfma_f32_16x16x32_bf16 a[248:251], a[152:155], v[4:7], 0\n\t"
    "v_exp_f32_e32 v14, v14\n\t"
    "v_mfma_f32_16x16x32_bf16 a[252:255], a[156:159], v[4:7], 0\n\t"
    "v_exp_f32_e32 v15, v15\n\t"
    "s_waitcnt lgkmcnt(0)\n\t"
    "v_cvt_pk_bf16_f32 v8, v8, v9\n\t"
    "v_cvt_pk_bf16_f32 v9, v10, v11\n\t"
    "v_cvt_pk_bf16_f32 v10, v12, v13\n\t"
    "v_cvt_pk_bf16_f32 v11, v14, v15\n\t"
    "v_lshlrev_b32_e32 v232, 16, v16\n\t"
    "v_and_b32_e32 v233, s66, v16\n\t"
    "v_mfma_f32_16x16x32_bf16 a[224:227], a[160:163], v[8:11], a[224:227]\n\t"
    "v_lshlrev_b32_e32 v234, 16, v17\n\t"
    "v_and_b32_e32 v235, s66, v17\n\t"
    "v_mfma_f32_16x16x32_bf16 a[228:231], a[164:167], v[8:11], a[228:231]\n\t"
    "v_lshlrev_b32_e32 v236, 16, v18\n\t"
    "v_and_b32_e32 v237, s66, v18\n\t"
    "v_lshlrev_b32_e32 v238, 16, v19\n\t"
    "v_and_b32_e32 v239, s66, v19\n\t"
    "v_pk_mul_f32 v[232:233], v[232:233], s[64:65]\n\t"
    "v_pk_mul_f32 v[234:235], v[234:235], s[64:65]\n\t"
    "v_pk_mul_f32 v[236:237], v[236:237], s[64:65]\n\t"
    "v_pk_mul_f32 v[238:239], v[238:239], s[64:65]\n\t"
    "v_mfma_f32_16x16x32_bf16 a[232:235], a[168:171], v[8:11], a[232:235]\n\t"
    "v_cvt_pk_bf16_f32 v232, v232, v233\n\t"
    "v_cvt_pk_bf16_f32 v234, v234, v235\n\t"
    "v_cvt_pk_bf16_f32 v236, v236, v237\n\t"
    "v_mfma_f32_16x16x32_bf16 a[236:239], a[172:175], v[8:11], a[236:239]\n\t"
    "v_cvt_pk_bf16_f32 v238, v238, v239\n\t"
    "v_accvgpr_write_b32 a80, v232\n\t"
    "v_accvgpr_write_b32 a81, v234\n\t"
    "v_accvgpr_write_b32 a82, v236\n\t"
    "v_accvgpr_write_b32 a83, v238\n\t"
    "v_lshlrev_b32_e32 v232, 16, v20\n\t"
    "v_and_b32_e32 v233, s66, v20\n\t"
    "v_mfma_f32_16x16x32_bf16 a[240:243], a[176:179], v[8:11], a[240:243]\n\t"
    "v_lshlrev_b32_e32 v234, 16, v21\n\t"
    "v_and_b32_e32 v235, s66, v21\n\t"
    "v_mfma_f32_16x16x32_bf16 a[244:247], a[180:183], v[8:11], a[244:247]\n\t"
    "v_lshlrev_b32_e32 v236, 16, v22\n\t"
    "v_and_b32_e32 v237, s66, v22\n\t"
    "v_lshlrev_b32_e32 v238, 16, v23\n\t"
    "v_and_b32_e32 v239, s66, v23\n\t"
    "v_pk_mul_f32 v[232:233], v[232:233], s[64:65]\n\t"
    "v_pk_mul_f32 v[234:235], v[234:235], s[64:65]\n\t"
    "v_pk_mul_f32 v[236:237], v[236:237], s[64:65]\n\t"
    "v_pk_mul_f32 v[238:239], v[238:239], s[64:65]\n\t"
    "v_mfma_f32_16x16x32_bf16 v[16:19], a[96:99], a[80:83], 0\n\t"
    "v_cvt_pk_bf16_f32 v232, v232, v233\n\t"
    "v_cvt_pk_bf16_f32 v234, v234, v235\n\t"
    "v_cvt_pk_bf16_f32 v236, v236, v237\n\t"
    "v_mfma_f32_16x16x32_bf16 v[20:23], a[112:115], a[80:83], 0\n\t"
    "v_cvt_pk_bf16_f32 v238, v238, v239\n\t"
    "v_accvgpr_write_b32 a84, v232\n\t"
    "v_accvgpr_write_b32 a85, v234\n\t"
    "v_accvgpr_write_b32 a86, v236\n\t"
    "v_accvgpr_write_b32 a87, v238\n\t"
    "v_lshlrev_b32_e32 v232, 16, v24\n\t"
    "v_and_b32_e32 v233, s66, v24\n\t"
    "v_mfma_f32_16x16x32_bf16 v[16:19], a[100:103], a[84:87], v[16:19]\n\t"
    "v_lshlrev_b32_e32 v234, 16, v25\n\t"
    "v_and_b32_e32 v235, s66, v25\n\t"
    "v_mfma_f32_16x16x32_bf16 v[20:23], a[116:119], a[84:87], v[20:23]\n\t"
    "v_lshlrev_b32_e32 v236, 16, v26\n\t"
    "v_and_b32_e32 v237, s66, v26\n\t"
    "v_lshlrev_b32_e32 v238, 16, v27\n\t"
    "v_and_b32_e32 v239, s66, v27\n\t"
    "v_pk_mul_f32 v[232:233], v[232:233], s[64:65]\n\t"
    "v_pk_mul_f32 v[234:235], v[234:235], s[64:65]\n\t"
    "v_pk_mul_f32 v[236:237], v[236:237], s[64:65]\n\t"
    "v_pk_mul_f32 v[238:239], v[238:239], s[64:65]\n\t"
    "v_mfma_f32_16x16x32_bf16 a[248:251], a[184:187], v[8:11], a[248:251]\n\t"
    "v_cvt_pk_bf16_f32 v232, v232, v233\n\t"
    "v_cvt_pk_bf16_f32 v234, v234, v235\n\t"
    "v_cvt_pk_bf16_f32 v236, v236, v237\n\t"
    "v_mfma_f32_16x16x32_bf16 a[252:255], a[188:191], v[8:11], a[252:255]\n\t"
    "v_cvt_pk_bf16_f32 v238, v238, v239\n\t"
    "v_accvgpr_write_b32 a88, v232\n\t"
    "v_accvgpr_write_b32 a89, v234\n\t"
    "v_accvgpr_write_b32 a90, v236\n\t"
    "v_accvgpr_write_b32 a91, v238\n\t"
    "v_lshlrev_b32_e32 v232, 16, v28\n\t"
    "v_and_b32_e32 v233, s66, v28\n\t"
    "v_mfma_f32_16x16x32_bf16 v[16:19], a[104:107], a[88:91], v[16:19]\n\t"
    "v_lshlrev_b32_e32 v234, 16, v29\n\t"
    "v_and_b32_e32 v235, s66, v29\n\t"
    "v_mfma_f32_16x16x32_bf16 v[20:23], a[120:123], a[88:91], v[20:23]\n\t"
    "v_lshlrev_b32_e32 v236, 16, v30\n\t"
    "v_and_b32_e32 v237, s66, v30\n\t"
    "v_lshlrev_b32_e32 v238, 16, v31\n\t"
    "v_and_b32_e32 v239, s66, v31\n\t"
    "v_pk_mul_f32 v[232:233], v[232:233], s[64:65]\n\t"
    "v_pk_mul_f32 v[234:235], v[234:235], s[64:65]\n\t"
    "v_pk_mul_f32 v[236:237], v[236:237], s[64:65]\n\t"
    "v_pk_mul_f32 v[238:239], v[238:239], s[64:65]\n\t"
    "v_smfmac_f32_16x16x64_bf16 v[64:67], v[68:71], v[4:11], v93\n\t"
    "v_cvt_pk_bf16_f32 v232, v232, v233\n\t"
    "v_cvt_pk_bf16_f32 v234, v234, v235\n\t"
    "v_cvt_pk_bf16_f32 v236, v236, v237\n\t"
    "v_cvt_pk_bf16_f32 v238, v238, v239\n\t"
    "v_accvgpr_write_b32 a92, v232\n\t"
    "v_accvgpr_write_b32 a93, v234\n\t"
    "v_accvgpr_write_b32 a94, v236\n\t"
    "v_accvgpr_write_b32 a95, v238\n\t"
    "s_nop 1\n\t"
    "v_mfma_f32_16x16x32_bf16 v[16:19], a[108:111], a[92:95], v[16:19]\n\t"
    "v_mfma_f32_16x16x32_bf16 v[20:23], a[124:127], a[92:95], v[20:23]\n\t"
    "s_add_u32 s56, s69, %[rot]\n\t"
    "s_add_u32 s56, s56, 0x8000\n\t"
    "s_and_b32 s56, s56, 0x3ffff\n\t"
    "s_add_u32 s57, s57, 0x10000\n\t"
    "s_mov_b32 m0, s57\n\t"
    "s_nop 0\n\t"
    "buffer_load_dwordx4 v88, s[40:43], s56 offen offset:0 nt lds\n\t"
    "buffer_load_dwordx4 v89, s[40:43], s56 offen offset:1024 nt lds\n\t"
    "buffer_load_dwordx4 v90, s[40:43], s56 offen offset:2048 nt lds\n\t"
    "buffer_load_dwordx4 v91, s[40:43], s56 offen offset:3072 nt lds\n\t"
    "s_bitset1_b32 m0, 14\n\t"
    "s_nop 0\n\t"
    "v_max3_f32 v240, v16, v17, v18\n\t"
    "v_max3_f32 v240, v240, v19, v20\n\t"
    "v_max3_f32 v240, v240, v21, v22\n\t"
    "v_max_f32_e32 v240, v240, v23\n\t"
    "v_mov_b32_e32 v241, v240\n\t"
    "s_nop 1\n\t"
    "v_permlane32_swap_b32 v240, v241\n\t"
    "v_max_f32_e32 v240, v240, v241\n\t"
    "v_mov_b32_e32 v241, v240\n\t"
    "s_nop 1\n\t"
    "v_permlane16_swap_b32 v240, v241\n\t"
    "v_max_f32_e32 v240, v240, v241\n\t"
    "v_sub_f32_e32 v52, 0, v240\n\t"
    "v_min_f32_e32 v52, s74, v52\n\t"
    "v_mov_b32_e32 v53, v52\n\t"
    "v_mov_b32_e32 v54, v52\n\t"
    "v_mov_b32_e32 v55, v52\n\t"
    "v_add_f32_e32 v23, v52, v23\n\t"
    "v_add_f32_e32 v22, v52, v22\n\t"
    "v_mfma_f32_16x16x32_bf16 v[24:27], v[192:195], a[80:83], v[52:55]\n\t"
    "v_add_f32_e32 v21, v52, v21\n\t"
    "v_add_f32_e32 v20, v52, v20\n\t"
    "v_mfma_f32_16x16x32_bf16 v[24:27], v[196:199], a[84:87], v[24:27]\n\t"
    "v_add_f32_e32 v19, v52, v19\n\t"
    "v_add_f32_e32 v18, v52, v18\n\t"
    "v_mfma_f32_16x16x32_bf16 v[24:27], v[200:203], a[88:91], v[24:27]\n\t"
    "v_add_f32_e32 v17, v52, v17\n\t"
    "v_add_f32_e32 v16, v52, v16\n\t"
    "v_mfma_f32_16x16x32_bf16 v[24:27], v[204:207], a[92:95], v[24:27]\n\t"
    "v_exp_f32_e32 v23, v23\n\t"
    "v_mfma_f32_16x16x32_bf16 v[28:31], v[208:211], a[80:83], v[52:55]\n\t"
    "v_exp_f32_e32 v22, v22\n\t"
    "v_mfma_f32_16x16x32_bf16 v[28:31], v[212:215], a[84:87], v[28:31]\n\t"
    "v_exp_f32_e32 v21, v21\n\t"
    "v_mfma_f32_16x16x32_bf16 v[28:31], v[216:219], a[88:91], v[28:31]\n\t"
    "v_exp_f32_e32 v20, v20\n\t"
    "v_mfma_f32_16x16x32_bf16 v[28:31], v[220:223], a[92:95], v[28:31]\n\t"
    "v_exp_f32_e32 v19, v19\n\t"
    "s_waitcnt vmcnt(10)\n\t"
    "s_barrier\n\t"
    "ds_read_b128 a[96:99], v76 offset:32768\n\t"
    "ds_read_b128 a[100:103], v77 offset:32768\n\t"
    "ds_read_b128 a[104:107], v78 offset:32768\n\t"
    "ds_read_b128 a[108:111], v79 offset:32768\n\t"
    "ds_read_b128 a[112:115], v76 offset:36864\n\t"
    "ds_read_b128 a[116:119], v77 offset:36864\n\t"
    "ds_read_b128 a[120:123], v78 offset:36864\n\t"
    "ds_read_b128 a[124:127], v79 offset:36864\n\t"
    "v_exp_f32_e32 v18, v18\n\t"
    "v_exp_f32_e32 v17, v17\n\t"
    "v_exp_f32_e32 v16, v16\n\t"
    "s_nop 0\n\t"
    "v_cvt_pk_bf16_f32 v23, v22, v23\n\t"
    "v_cvt_pk_bf16_f32 v22, v20, v21\n\t"
    "v_cvt_pk_bf16_f32 v21, v18, v19\n\t"
    "v_cvt_pk_bf16_f32 v20, v16, v17\n\t"
    "s_waitcnt lgkmcnt(4)\n\t"
    "v_mfma_f32_16x16x32_bf16 v[0:3], a[96:99], a[0:3], v[32:35]\n\t"
    "v_exp_f32_e32 v24, v24\n\t"
    "v_mfma_f32_16x16x32_bf16 v[0:3], a[100:103], a[4:7], v[0:3]\n\t"
    "v_mfma_f32_16x16x32_bf16 v[0:3], a[104:107], a[8:11], v[0:3]\n\t"
    "v_exp_f32_e32 v25, v25\n\t"
    "v_mfma_f32_16x16x32_bf16 v[0:3], a[108:111], a[12:15], v[0:3]\n\t"
    "s_waitcnt lgkmcnt(0)\n\t"
    "v_cvt_pk_bf16_f32 v24, v24, v25\n\t"
    "v_mfma_f32_16x16x32_bf16 v[4:7], a[112:115], a[0:3], v[32:35]\n\t"
    "v_exp_f32_e32 v26, v26\n\t"
    "v_mfma_f32_16x16x32_bf16 v[4:7], a[116:119], a[4:7], v[4:7]\n\t"
    "v_mfma_f32_16x16x32_bf16 v[4:7], a[120:123], a[8:11], v[4:7]\n\t"
    "v_exp_f32_e32 v27, v27\n\t"
    "v_mfma_f32_16x16x32_bf16 v[4:7], a[124:127], a[12:15], v[4:7]\n\t"
    "v_cvt_pk_bf16_f32 v25, v26, v27\n\t"
    "v_mfma_f32_16x16x32_bf16 v[192:195], a[128:131], v[20:23], 0\n\t"
    "v_exp_f32_e32 v28, v28\n\t"
    "v_mfma_f32_16x16x32_bf16 v[196:199], a[132:135], v[20:23], 0\n\t"
    "s_waitcnt vmcnt(8)\n\t"
    "s_barrier\n\t"
    "v_mfma_f32_16x16x32_bf16 v[200:203], a[136:139], v[20:23], 0\n\t"
    "v_exp_f32_e32 v29, v29\n\t"
    "v_mfma_f32_16x16x32_bf16 v[204:207], a[140:143], v[20:23], 0\n\t"
    "ds_read_b64_tr_b16 a[128:129], v80 offset:32768\n\t"
    "v_cvt_pk_bf16_f32 v26, v28, v29\n\t"
    "v_mfma_f32_16x16x32_bf16 v[208:211], a[144:147], v[20:23], 0\n\t"
    "v_exp_f32_e32 v30, v30\n\t"
    "v_mfma_f32_16x16x32_bf16 v[212:215], a[148:151], v[20:23], 0\n\t"
    "ds_read_b64_tr_b16 a[130:131], v80 offset:36864\n\t"
    "v_mfma_f32_16x16x32_bf16 v[216:219], a[152:155], v[20:23], 0\n\t"
    "v_exp_f32_e32 v31, v31\n\t"
    "v_mfma_f32_16x16x32_bf16 v[220:223], a[156:159], v[20:23], 0\n\t"
    "ds_read_b64_tr_b16 a[132:133], v80 offset:32776\n\t"
    "v_cvt_pk_bf16_f32 v27, v30, v31\n\t"
    "v_mfma_f32_16x16x32_bf16 v[8:11], a[96:99], a[16:19], v[36:39]\n\t"
    "v_exp_f32_e32 v7, v7\n\t"
    "v_mfma_f32_16x16x32_bf16 v[8:11], a[100:103], a[20:23], v[8:11]\n\t"
    "ds_read_b64_tr_b16 a[134:135], v80 offset:36872\n\t"
    "v_mfma_f32_16x16x32_bf16 v[8:11], a[104:107], a[24:27], v[8:11]\n\t"
    "v_exp_f32_e32 v6, v6\n\t"
    "v_mfma_f32_16x16x32_bf16 v[8:11], a[108:111], a[28:31], v[8:11]\n\t"
    "ds_read_b64_tr_b16 a[136:137], v81 offset:32768\n\t"
    "v_cvt_pk_bf16_f32 v7, v6, v7\n\t"
    "v_mfma_f32_16x16x32_bf16 v[12:15], a[112:115], a[16:19], v[36:39]\n\t"
    "v_exp_f32_e32 v5, v5\n\t"
    "v_mfma_f32_16x16x32_bf16 v[12:15], a[116:119], a[20:23], v[12:15]\n\t"
    "ds_read_b64_tr_b16 a[138:139], v81 offset:36864\n\t"
    "ds_read_b64_tr_b16 a[140:141], v81 offset:32776\n\t"
    "v_mfma_f32_16x16x32_bf16 v[12:15], a[120:123], a[24:27], v[12:15]\n\t"
    "v_exp_f32_e32 v4, v4\n\t"
    "v_mfma_f32_16x16x32_bf16 v[12:15], a[124:127], a[28:31], v[12:15]\n\t"
    "ds_read_b64_tr_b16 a[142:143], v81 offset:36872\n\t"
    "v_cvt_pk_bf16_f32 v6, v4, v5\n\t"
    "v_mfma_f32_16x16x32_bf16 v[192:195], a[160:163], v[24:27], v[192:195]\n\t"
    "v_exp_f32_e32 v3, v3\n\t"
    "v_mfma_f32_16x16x32_bf16 v[196:199], a[164:167], v[24:27], v[196:199]\n\t"
    "ds_read_b64_tr_b16 a[144:145], v82 offset:32768\n\t"
    "ds_read_b64_tr_b16 a[146:147], v82 offset:36864\n\t"
    "v_mfma_f32_16x16x32_bf16 v[200:203], a[168:171], v[24:27], v[200:203]\n\t"
    "v_exp_f32_e32 v2, v2\n\t"
    "v_mfma_f32_16x16x32_bf16 v[204:207], a[172:175], v[24:27], v[204:207]\n\t"
    "ds_read_b64_tr_b16 a[148:149], v82 offset:32776\n\t"
    "v_cvt_pk_bf16_f32 v5, v2, v3\n\t"
    "v_mfma_f32_16x16x32_bf16 v[208:211], a[176:179], v[24:27], v[208:211]\n\t"
    "v_exp_f32_e32 v1, v1\n\t"
    "v_mfma_f32_16x16x32_bf16 v[212:215], a[180:183], v[24:27], v[212:215]\n\t"
    "ds_read_b64_tr_b16 a[150:151], v82 offset:36872\n\t"
    "ds_read_b64_tr_b16 a[152:153], v83 offset:32768\n\t"
    "v_mfma_f32_16x16x32_bf16 v[216:219], a[184:187], v[24:27], v[216:219]\n\t"
    "v_exp_f32_e32 v0, v0\n\t"
    "v_mfma_f32_16x16x32_bf16 v[220:223], a[188:191], v[24:27], v[220:223]\n\t"
    "ds_read_b64_tr_b16 a[154:155], v83 offset:36864\n\t"
    "v_cvt_pk_bf16_f32 v4, v0, v1\n\t"
    "v_smfmac_f32_16x16x64_bf16 v[64:67], v[252:255], v[20:27], v93\n\t"
    "ds_read_b64_tr_b16 a[156:157], v83 offset:32776\n\t"
    "ds_read_b64_tr_b16 a[158:159], v83 offset:36872\n\t"
    "v_mfma_f32_16x16x32_bf16 v[16:19], a[96:99], a[32:35], v[40:43]\n\t"
    "v_exp_f32_e32 v8, v8\n\t"
    "v_mfma_f32_16x16x32_bf16 v[16:19], a[100:103], a[36:39], v[16:19]\n\t"
    "buffer_load_dwordx4 v92, s[44:47], s56 offen offset:0 nt lds\n\t"
    "v_mfma_f32_16x16x32_bf16 v[16:19], a[104:107], a[40:43], v[16:19]\n\t"
    "v_exp_f32_e32 v9, v9\n\t"
    "v_mfma_f32_16x16x32_bf16 v[16:19], a[108:111], a[44:47], v[16:19]\n\t"
    "v_cvt_pk_bf16_f32 v8, v8, v9\n\t"
    "v_mbcnt_lo_u32_b32 v240, -1, 0\n\t"
    "v_mfma_f32_16x16x32_bf16 v[20:23], a[112:115], a[32:35], v[40:43]\n\t"
    "v_exp_f32_e32 v10, v10\n\t"
    "v_mfma_f32_16x16x32_bf16 v[20:23], a[116:119], a[36:39], v[20:23]\n\t"
    "buffer_load_dwordx4 v92, s[44:47], s56 offen offset:1024 nt lds\n\t"
    "v_mfma_f32_16x16x32_bf16 v[20:23], a[120:123], a[40:43], v[20:23]\n\t"
    "v_exp_f32_e32 v11, v11\n\t"
    "v_mfma_f32_16x16x32_bf16 v[20:23], a[124:127], a[44:47], v[20:23]\n\t"
    "v_cvt_pk_bf16_f32 v9, v10, v11\n\t"
    "s_waitcnt lgkmcnt(0)\n\t"
    "v_mfma_f32_16x16x32_bf16 v[96:99], a[128:131], v[4:7], v[96:99]\n\t"
    "v_exp_f32_e32 v12, v12\n\t"
    "v_mfma_f32_16x16x32_bf16 v[100:103], a[132:135], v[4:7], v[100:103]\n\t"
    "buffer_load_dwordx4 v92, s[44:47], s56 offen offset:2048 nt lds\n\t"
    "v_mfma_f32_16x16x32_bf16 v[104:107], a[136:139], v[4:7], v[104:107]\n\t"
    "v_exp_f32_e32 v13, v13\n\t"
    "v_mfma_f32_16x16x32_bf16 v[108:111], a[140:143], v[4:7], v[108:111]\n\t"
    "v_cvt_pk_bf16_f32 v10, v12, v13\n\t"
    "v_mbcnt_hi_u32_b32 v240, -1, v240\n\t"
    "v_mfma_f32_16x16x32_bf16 v[112:115], a[144:147], v[4:7], v[112:115]\n\t"
    "v_exp_f32_e32 v14, v14\n\t"
    "v_mfma_f32_16x16x32_bf16 v[116:119], a[148:151], v[4:7], v[116:119]\n\t"
    "buffer_load_dwordx4 v92, s[44:47], s56 offen offset:3072 nt lds\n\t"
    "v_mfma_f32_16x16x32_bf16 v[120:123], a[152:155], v[4:7], v[120:123]\n\t"
    "v_exp_f32_e32 v15, v15\n\t"
    "v_mfma_f32_16x16x32_bf16 v[124:127], a[156:159], v[4:7], v[124:127]\n\t"
    "v_cvt_pk_bf16_f32 v11, v14, v15\n\t"
    "s_add_u32 s56, s56, 0x4000\n\t"
    "v_mfma_f32_16x16x32_bf16 v[24:27], a[96:99], a[48:51], v[44:47]\n\t"
    "v_exp_f32_e32 v23, v23\n\t"
    "v_mfma_f32_16x16x32_bf16 v[24:27], a[100:103], a[52:55], v[24:27]\n\t"
    "s_and_b32 s56, s56, 0x3ffff\n\t"
    "s_add_u32 s57, s57, s60\n\t"
    "v_mfma_f32_16x16x32_bf16 v[24:27], a[104:107], a[56:59], v[24:27]\n\t"
    "v_exp_f32_e32 v22, v22\n\t"
    "v_mfma_f32_16x16x32_bf16 v[24:27], a[108:111], a[60:63], v[24:27]\n\t"
    "v_cvt_pk_bf16_f32 v23, v22, v23\n\t"
    "s_cmp_le_u32 s58, 12\n\t"
    "v_mfma_f32_16x16x32_bf16 v[28:31], a[112:115], a[48:51], v[44:47]\n\t"
    "v_exp_f32_e32 v21, v21\n\t"
    "v_mfma_f32_16x16x32_bf16 v[28:31], a[116:119], a[52:55], v[28:31]\n\t"
    "s_cselect_b32 s42, 0x40000, 0\n\t"
    "s_mov_b32 s46, s42\n\t"
    "v_mfma_f32_16x16x32_bf16 v[28:31], a[120:123], a[56:59], v[28:31]\n\t"
    "v_exp_f32_e32 v20, v20\n\t"
    "v_mfma_f32_16x16x32_bf16 v[28:31], a[124:127], a[60:63], v[28:31]\n\t"
    "v_cvt_pk_bf16_f32 v22, v20, v21\n\t"
    "s_mov_b32 m0, s57\n\t"
    "v_mfma_f32_16x16x32_bf16 v[128:131], a[128:131], v[8:11], v[128:131]\n\t"
    "v_exp_f32_e32 v19, v19\n\t"
    "v_mfma_f32_16x16x32_bf16 v[132:135], a[132:135], v[8:11], v[132:135]\n\t"
    "ds_write_b128 v86, a[80:83] offset:16384\n\t"
    "v_mfma_f32_16x16x32_bf16 v[136:139], a[136:139], v[8:11], v[136:139]\n\t"
    "v_exp_f32_e32 v18, v18\n\t"
    "v_mfma_f32_16x16x32_bf16 v[140:143], a[140:143], v[8:11], v[140:143]\n\t"
    "v_cvt_pk_bf16_f32 v21, v18, v19\n\t"
    "s_add_u32 s58, s58, 1\n\t"
    "v_mfma_f32_16x16x32_bf16 v[144:147], a[144:147], v[8:11], v[144:147]\n\t"
    "v_exp_f32_e32 v17, v17\n\t"
    "v_mfma_f32_16x16x32_bf16 v[148:151], a[148:151], v[8:11], v[148:151]\n\t"
    "ds_write_b128 v86, a[84:87] offset:17408\n\t"
    "v_mfma_f32_16x16x32_bf16 v[152:155], a[152:155], v[8:11], v[152:155]\n\t"
    "v_exp_f32_e32 v16, v16\n\t"
    "v_mfma_f32_16x16x32_bf16 v[156:159], a[156:159], v[8:11], v[156:159]\n\t"
    "v_cvt_pk_bf16_f32 v20, v16, v17\n\t"
    "s_and_b32 s61, s58, 3\n\t"
    "v_smfmac_f32_16x16x64_bf16 v[56:59], v[72:75], v[4:11], v93\n\t"
    "s_cmp_eq_u32 s61, 3\n\t"
    "s_cselect_b32 s59, s63, 0x8000\n\t"
    "v_mfma_f32_16x16x32_bf16 v[0:3], a[96:99], a[64:67], v[48:51]\n\t"
    "v_exp_f32_e32 v24, v24\n\t"
    "v_mfma_f32_16x16x32_bf16 v[0:3], a[100:103], a[68:71], v[0:3]\n\t"
    "buffer_load_dwordx4 v88, s[40:43], s56 offen offset:0 nt lds\n\t"
    "v_mfma_f32_16x16x32_bf16 v[0:3], a[104:107], a[72:75], v[0:3]\n\t"
    "v_exp_f32_e32 v25, v25\n\t"
    "v_mfma_f32_16x16x32_bf16 v[0:3], a[108:111], a[76:79], v[0:3]\n\t"
    "v_cvt_pk_bf16_f32 v24, v24, v25\n\t"
    "s_cmp_eq_u32 s61, 1\n\t"
    "v_mfma_f32_16x16x32_bf16 v[4:7], a[112:115], a[64:67], v[48:51]\n\t"
    "v_exp_f32_e32 v26, v26\n\t"
    "v_mfma_f32_16x16x32_bf16 v[4:7], a[116:119], a[68:71], v[4:7]\n\t"
    "buffer_load_dwordx4 v89, s[40:43], s56 offen offset:1024 nt lds\n\t"
    "v_mfma_f32_16x16x32_bf16 v[4:7], a[120:123], a[72:75], v[4:7]\n\t"
    "v_exp_f32_e32 v27, v27\n\t"
    "v_mfma_f32_16x16x32_bf16 v[4:7], a[124:127], a[76:79], v[4:7]\n\t"
    "v_cvt_pk_bf16_f32 v25, v26, v27\n\t"
    "s_cselect_b32 s60, s63, 0x8000\n\t"
    "v_mfma_f32_16x16x32_bf16 v[160:163], a[128:131], v[20:23], v[160:163]\n\t"
    "v_exp_f32_e32 v28, v28\n\t"
    "v_mfma_f32_16x16x32_bf16 v[164:167], a[132:135], v[20:23], v[164:167]\n\t"
    "buffer_load_dwordx4 v90, s[40:43], s56 offen offset:2048 nt lds\n\t"
    "v_mfma_f32_16x16x32_bf16 v[168:171], a[136:139], v[20:23], v[168:171]\n\t"
    "v_exp_f32_e32 v29, v29\n\t"
    "v_mfma_f32_16x16x32_bf16 v[172:175], a[140:143], v[20:23], v[172:175]\n\t"
    "v_cvt_pk_bf16_f32 v26, v28, v29\n\t"
    "s_cmp_eq_u32 s58, 14\n\t"
    "v_mfma_f32_16x16x32_bf16 v[176:179], a[144:147], v[20:23], v[176:179]\n\t"
    "v_exp_f32_e32 v30, v30\n\t"
    "v_mfma_f32_16x16x32_bf16 v[180:183], a[148:151], v[20:23], v[180:183]\n\t"
    "s_waitcnt vmcnt(11)\n\t"
    "s_barrier\n\t"
    "v_mfma_f32_16x16x32_bf16 v[184:187], a[152:155], v[20:23], v[184:187]\n\t"
    "v_exp_f32_e32 v31, v31\n\t"
    "v_mfma_f32_16x16x32_bf16 v[188:191], a[156:159], v[20:23], v[188:191]\n\t"
    "ds_read_b64_tr_b16 a[160:161], v80 offset:40960\n\t"
    "v_cvt_pk_bf16_f32 v27, v30, v31\n\t"
    "v_mfma_f32_16x16x32_bf16 v[8:11], a[96:99], a[80:83], v[52:55]\n\t"
    "v_exp_f32_e32 v7, v7\n\t"
    "v_mfma_f32_16x16x32_bf16 v[8:11], a[100:103], a[84:87], v[8:11]\n\t"
    "ds_read_b128 a[96:99], v76 offset:40960\n\t"
    "ds_read_b64_tr_b16 a[162:163], v80 offset:45056\n\t"
    "v_mfma_f32_16x16x32_bf16 v[8:11], a[104:107], a[88:91], v[8:11]\n\t"
    "v_exp_f32_e32 v6, v6\n\t"
    "v_mfma_f32_16x16x32_bf16 v[8:11], a[108:111], a[92:95], v[8:11]\n\t"
    "ds_read_b128 a[100:103], v77 offset:40960\n\t"
    "v_cvt_pk_bf16_f32 v7, v6, v7\n\t"
    "v_mfma_f32_16x16x32_bf16 v[12:15], a[112:115], a[80:83], v[52:55]\n\t"
    "v_exp_f32_e32 v5, v5\n\t"
    "v_mfma_f32_16x16x32_bf16 v[12:15], a[116:119], a[84:87], v[12:15]\n\t"
    "ds_read_b128 a[104:107], v78 offset:40960\n\t"
    "ds_read_b128 a[108:111], v79 offset:40960\n\t"
    "v_mfma_f32_16x16x32_bf16 v[12:15], a[120:123], a[88:91], v[12:15]\n\t"
    "v_exp_f32_e32 v4, v4\n\t"
    "v_mfma_f32_16x16x32_bf16 v[12:15], a[124:127], a[92:95], v[12:15]\n\t"
    "ds_read_b128 a[112:115], v76 offset:45056\n\t"
    "v_cvt_pk_bf16_f32 v6, v4, v5\n\t"
    "v_mfma_f32_16x16x32_bf16 a[192:195], a[128:131], v[24:27], a[192:195]\n\t"
    "v_exp_f32_e32 v3, v3\n\t"
    "v_mfma_f32_16x16x32_bf16 a[196:199], a[132:135], v[24:27], a[196:199]\n\t"
    "ds_read_b128 a[116:119], v77 offset:45056\n\t"
    "ds_read_b128 a[120:123], v78 offset:45056\n\t"
    "v_mfma_f32_16x16x32_bf16 a[200:203], a[136:139], v[24:27], a[200:203]\n\t"
    "v_exp_f32_e32 v2, v2\n\t"
    "v_mfma_f32_16x16x32_bf16 a[204:207], a[140:143], v[24:27], a[204:207]\n\t"
    "ds_read_b128 a[124:127], v79 offset:45056\n\t"
    "v_cvt_pk_bf16_f32 v5, v2, v3\n\t"
    "v_mfma_f32_16x16x32_bf16 a[208:211], a[144:147], v[24:27], a[208:211]\n\t"
    "v_exp_f32_e32 v1, v1\n\t"
    "v_mfma_f32_16x16x32_bf16 a[212:215], a[148:151], v[24:27], a[212:215]\n\t"
    "buffer_load_dwordx4 v91, s[40:43], s56 offen offset:3072 nt lds\n\t"
    "v_mfma_f32_16x16x32_bf16 a[216:219], a[152:155], v[24:27], a[216:219]\n\t"
    "v_exp_f32_e32 v0, v0\n\t"
    "v_mfma_f32_16x16x32_bf16 a[220:223], a[156:159], v[24:27], a[220:223]\n\t"
    "v_cvt_pk_bf16_f32 v4, v0, v1\n\t"
    "s_bitset1_b32 m0, 14\n\t"
    "v_smfmac_f32_16x16x64_bf16 v[60:63], v[72:75], v[20:27], v93\n\t"
    "ds_read_b64_tr_b16 a[164:165], v80 offset:40968\n\t"
    "s_waitcnt lgkmcnt(3)\n\t"
    "v_mfma_f32_16x16x32_bf16 v[16:19], a[96:99], a[0:3], v[32:35]\n\t"
    "v_exp_f32_e32 v8, v8\n\t"
    "v_mfma_f32_16x16x32_bf16 v[16:19], a[100:103], a[4:7], v[16:19]\n\t"
    "ds_read_b64_tr_b16 a[166:167], v80 offset:45064\n\t"
    "s_cselect_b32 s59, 0, s59\n\t"
    "v_mfma_f32_16x16x32_bf16 v[16:19], a[104:107], a[8:11], v[16:19]\n\t"
    "v_exp_f32_e32 v9, v9\n\t"
    "v_mfma_f32_16x16x32_bf16 v[16:19], a[108:111], a[12:15], v[16:19]\n\t"
    "ds_read_b64_tr_b16 a[168:169], v81 offset:40960\n\t"
    "v_cvt_pk_bf16_f32 v8, v8, v9\n\t"
    "v_mfma_f32_16x16x32_bf16 v[20:23], a[112:115], a[0:3], v[32:35]\n\t"
    "v_exp_f32_e32 v10, v10\n\t"
    "v_mfma_f32_16x16x32_bf16 v[20:23], a[116:119], a[4:7], v[20:23]\n\t"
    "ds_read_b64_tr_b16 a[170:171], v81 offset:45056\n\t"
    "s_waitcnt lgkmcnt(4)\n\t"
    "v_mfma_f32_16x16x32_bf16 v[20:23], a[120:123], a[8:11], v[20:23]\n\t"
    "v_exp_f32_e32 v11, v11\n\t"
    "v_mfma_f32_16x16x32_bf16 v[20:23], a[124:127], a[12:15], v[20:23]\n\t"
    "ds_read_b64_tr_b16 a[172:173], v81 offset:40968\n\t"
    "v_cvt_pk_bf16_f32 v9, v10, v11\n\t"
    "v_mfma_f32_16x16x32_bf16 a[224:227], a[128:131], v[4:7], a[224:227]\n\t"
    "v_exp_f32_e32 v12, v12\n\t"
    "v_mfma_f32_16x16x32_bf16 a[228:231], a[132:135], v[4:7], a[228:231]\n\t"
    "ds_read_b64_tr_b16 a[174:175], v81 offset:45064\n\t"
    "v_and_b32_e32 v240, 3, v240\n\t"
    "v_mfma_f32_16x16x32_bf16 a[232:235], a[136:139], v[4:7], a[232:235]\n\t"
    "v_exp_f32_e32 v13, v13\n\t"
    "v_mfma_f32_16x16x32_bf16 a[236:239], a[140:143], v[4:7], a[236:239]\n\t"
    "ds_read_b64_tr_b16 a[176:177], v82 offset:40960\n\t"
    "v_cvt_pk_bf16_f32 v10, v12, v13\n\t"
    "v_mfma_f32_16x16x32_bf16 a[240:243], a[144:147], v[4:7], a[240:243]\n\t"
    "v_exp_f32_e32 v14, v14\n\t"
    "v_mfma_f32_16x16x32_bf16 a[244:247], a[148:151], v[4:7], a[244:247]\n\t"
    "ds_read_b64_tr_b16 a[178:179], v82 offset:45056\n\t"
    "v_mov_b32_e32 v241, 0x3f803f80\n\t"
    "v_mfma_f32_16x16x32_bf16 a[248:251], a[152:155], v[4:7], a[248:251]\n\t"
    "v_exp_f32_e32 v15, v15\n\t"
    "v_mfma_f32_16x16x32_bf16 a[252:255], a[156:159], v[4:7], a[252:255]\n\t"
    "ds_read_b64_tr_b16 a[180:181], v82 offset:40968\n\t"
    "v_cvt_pk_bf16_f32 v11, v14, v15\n\t"
    "v_mfma_f32_16x16x32_bf16 v[24:27], a[96:99], a[16:19], v[36:39]\n\t"
    "v_exp_f32_e32 v23, v23\n\t"
    "v_mfma_f32_16x16x32_bf16 v[24:27], a[100:103], a[20:23], v[24:27]\n\t"
    "ds_read_b64_tr_b16 a[182:183], v82 offset:45064\n\t"
    "v_cmp_eq_u32_e32 vcc, 0, v240\n\t"
    "v_mfma_f32_16x16x32_bf16 v[24:27], a[104:107], a[24:27], v[24:27]\n\t"
    "v_exp_f32_e32 v22, v22\n\t"
    "v_mfma_f32_16x16x32_bf16 v[24:27], a[108:111], a[28:31], v[24:27]\n\t"
    "ds_read_b64_tr_b16 a[184:185], v83 offset:40960\n\t"
    "v_cvt_pk_bf16_f32 v23, v22, v23\n\t"
    "v_mfma_f32_16x16x32_bf16 v[28:31], a[112:115], a[16:19], v[36:39]\n\t"
    "v_exp_f32_e32 v21, v21\n\t"
    "v_mfma_f32_16x16x32_bf16 v[28:31], a[116:119], a[20:23], v[28:31]\n\t"
    "ds_read_b64_tr_b16 a[186:187], v83 offset:45056\n\t"
    "s_nop 1\n\t"
    "v_mfma_f32_16x16x32_bf16 v[28:31], a[120:123], a[24:27], v[28:31]\n\t"
    "v_exp_f32_e32 v20, v20\n\t"
    "v_mfma_f32_16x16x32_bf16 v[28:31], a[124:127], a[28:31], v[28:31]\n\t"
    "ds_read_b64_tr_b16 a[188:189], v83 offset:40968\n\t"
    "v_cvt_pk_bf16_f32 v22, v20, v21\n\t"
    "v_mfma_f32_16x16x32_bf16 v[192:195], a[128:131], v[8:11], v[192:195]\n\t"
    "v_exp_f32_e32 v19, v19\n\t"
    "v_mfma_f32_16x16x32_bf16 v[196:199], a[132:135], v[8:11], v[196:199]\n\t"
    "ds_read_b64_tr_b16 a[190:191], v83 offset:45064\n\t"
    "v_cndmask_b32_e64 v232, 0, v241, vcc\n\t"
    "v_mfma_f32_16x16x32_bf16 v[200:203], a[136:139], v[8:11], v[200:203]\n\t"
    "v_exp_f32_e32 v18, v18\n\t"
    "v_mfma_f32_16x16x32_bf16 v[204:207], a[140:143], v[8:11], v[204:207]\n\t"
    "v_cvt_pk_bf16_f32 v21, v18, v19\n\t"
    "v_cndmask_b32_e64 v233, 0, v241, vcc\n\t"
    "v_mfma_f32_16x16x32_bf16 v[208:211], a[144:147], v[8:11], v[208:211]\n\t"
    "v_exp_f32_e32 v17, v17\n\t"
    "v_mfma_f32_16x16x32_bf16 v[212:215], a[148:151], v[8:11], v[212:215]\n\t"
    "v_cndmask_b32_e64 v234, 0, v241, vcc\n\t"
    "v_cndmask_b32_e64 v235, 0, v241, vcc\n\t"
    "v_mfma_f32_16x16x32_bf16 v[216:219], a[152:155], v[8:11], v[216:219]\n\t"
    "v_exp_f32_e32 v16, v16\n\t"
    "v_mfma_f32_16x16x32_bf16 v[220:223], a[156:159], v[8:11], v[220:223]\n\t"
    "v_cvt_pk_bf16_f32 v20, v16, v17\n\t"
    "v_cmp_eq_u32_e32 vcc, 2, v240\n\t"
    "v_smfmac_f32_16x16x64_bf16 v[64:67], v[72:75], v[4:11], v93\n\t"
    "s_nop 1\n\t"
    "v_cndmask_b32_e64 v236, 0, v241, vcc\n\t"
    "v_mfma_f32_16x16x32_bf16 v[0:3], a[96:99], a[32:35], v[40:43]\n\t"
    "v_exp_f32_e32 v24, v24\n\t"
    "v_mfma_f32_16x16x32_bf16 v[0:3], a[100:103], a[36:39], v[0:3]\n\t"
    "buffer_load_dwordx4 v92, s[44:47], s56 offen offset:0 nt lds\n\t"
    "v_mfma_f32_16x16x32_bf16 v[0:3], a[104:107], a[40:43], v[0:3]\n\t"
    "v_exp_f32_e32 v25, v25\n\t"
    "v_mfma_f32_16x16x32_bf16 v[0:3], a[108:111], a[44:47], v[0:3]\n\t"
    "v_cvt_pk_bf16_f32 v24, v24, v25\n\t"
    "v_add_u32_e32 v76, 0x10000, v76\n\t"
    "v_mfma_f32_16x16x32_bf16 v[4:7], a[112:115], a[32:35], v[40:43]\n\t"
    "v_exp_f32_e32 v26, v26\n\t"
    "v_mfma_f32_16x16x32_bf16 v[4:7], a[116:119], a[36:39], v[4:7]\n\t"
    "buffer_load_dwordx4 v92, s[44:47], s56 offen offset:1024 nt lds\n\t"
    "v_mfma_f32_16x16x32_bf16 v[4:7], a[120:123], a[40:43], v[4:7]\n\t"
    "v_exp_f32_e32 v27, v27\n\t"
    "v_mfma_f32_16x16x32_bf16 v[4:7], a[124:127], a[44:47], v[4:7]\n\t"
    "v_cvt_pk_bf16_f32 v25, v26, v27\n\t"
    "s_waitcnt lgkmcnt(0)\n\t"
    "v_mfma_f32_16x16x32_bf16 v[96:99], a[160:163], v[20:23], v[96:99]\n\t"
    "v_exp_f32_e32 v28, v28\n\t"
    "v_mfma_f32_16x16x32_bf16 v[100:103], a[164:167], v[20:23], v[100:103]\n\t"
    "buffer_load_dwordx4 v92, s[44:47], s56 offen offset:2048 nt lds\n\t"
    "v_mfma_f32_16x16x32_bf16 v[104:107], a[168:171], v[20:23], v[104:107]\n\t"
    "v_exp_f32_e32 v29, v29\n\t"
    "v_mfma_f32_16x16x32_bf16 v[108:111], a[172:175], v[20:23], v[108:111]\n\t"
    "v_cvt_pk_bf16_f32 v26, v28, v29\n\t"
    "v_add_u32_e32 v77, 0x10000, v77\n\t"
    "v_mfma_f32_16x16x32_bf16 v[112:115], a[176:179], v[20:23], v[112:115]\n\t"
    "v_exp_f32_e32 v30, v30\n\t"
    "v_mfma_f32_16x16x32_bf16 v[116:119], a[180:183], v[20:23], v[116:119]\n\t"
    "buffer_load_dwordx4 v92, s[44:47], s56 offen offset:3072 nt lds\n\t"
    "v_mfma_f32_16x16x32_bf16 v[120:123], a[184:187], v[20:23], v[120:123]\n\t"
    "v_exp_f32_e32 v31, v31\n\t"
    "v_mfma_f32_16x16x32_bf16 v[124:127], a[188:191], v[20:23], v[124:127]\n\t"
    "v_cvt_pk_bf16_f32 v27, v30, v31\n\t"
    "v_add_u32_e32 v78, 0x10000, v78\n\t"
    "v_min_u32_e32 v242, 1, v242\n\t"
    "v_mul_u32_u24_e32 v242, 0x5ffc, v242\n\t"
    "v_add_u32_e32 v242, s71, v242\n\t"
    "v_min_u32_e32 v242, 0x16ffc, v242\n\t"
    "buffer_store_dword v242, v242, s[52:55], 0 offen sc1\n\t"
    "v_mfma_f32_16x16x32_bf16 v[8:11], a[96:99], a[48:51], v[44:47]\n\t"
    "v_exp_f32_e32 v7, v7\n\t"
    "v_mfma_f32_16x16x32_bf16 v[8:11], a[100:103], a[52:55], v[8:11]\n\t"
    "ds_write_b128 v86, a[88:91] offset:18432\n\t"
    "v_mfma_f32_16x16x32_bf16 v[8:11], a[104:107], a[56:59], v[8:11]\n\t"
    "v_exp_f32_e32 v6, v6\n\t"
    "v_mfma_f32_16x16x32_bf16 v[8:11], a[108:111], a[60:63], v[8:11]\n\t"
    "v_cvt_pk_bf16_f32 v7, v6, v7\n\t"
    "v_add_u32_e32 v79, 0x10000, v79\n\t"
    "v_mfma_f32_16x16x32_bf16 v[12:15], a[112:115], a[48:51], v[44:47]\n\t"
    "v_exp_f32_e32 v5, v5\n\t"
    "v_mfma_f32_16x16x32_bf16 v[12:15], a[116:119], a[52:55], v[12:15]\n\t"
    "ds_write_b128 v86, a[92:95] offset:19456\n\t"
    "v_mfma_f32_16x16x32_bf16 v[12:15], a[120:123], a[56:59], v[12:15]\n\t"
    "v_exp_f32_e32 v4, v4\n\t"
    "v_mfma_f32_16x16x32_bf16 v[12:15], a[124:127], a[60:63], v[12:15]\n\t"
    "v_cvt_pk_bf16_f32 v6, v4, v5\n\t"
    "v_add_u32_e32 v80, 0x10000, v80\n\t"
    "v_mfma_f32_16x16x32_bf16 v[128:131], a[160:163], v[24:27], v[128:131]\n\t"
    "v_exp_f32_e32 v3, v3\n\t"
    "v_mfma_f32_16x16x32_bf16 v[132:135], a[164:167], v[24:27], v[132:135]\n\t"
    "ds_write_b128 v86, v[52:55] offset:2048\n\t"
    "v_mfma_f32_16x16x32_bf16 v[136:139], a[168:171], v[24:27], v[136:139]\n\t"
    "v_exp_f32_e32 v2, v2\n\t"
    "v_mfma_f32_16x16x32_bf16 v[140:143], a[172:175], v[24:27], v[140:143]\n\t"
    "v_cvt_pk_bf16_f32 v5, v2, v3\n\t"
    "v_add_u32_e32 v81, 0x10000, v81\n\t"
    "v_mfma_f32_16x16x32_bf16 v[144:147], a[176:179], v[24:27], v[144:147]\n\t"
    "v_exp_f32_e32 v1, v1\n\t"
    "v_mfma_f32_16x16x32_bf16 v[148:151], a[180:183], v[24:27], v[148:151]\n\t"
    "v_add_u32_e32 v82, 0x10000, v82\n\t"
    "v_add_u32_e32 v83, 0x10000, v83\n\t"
    "v_mfma_f32_16x16x32_bf16 v[152:155], a[184:187], v[24:27], v[152:155]\n\t"
    "v_exp_f32_e32 v0, v0\n\t"
    "v_mfma_f32_16x16x32_bf16 v[156:159], a[188:191], v[24:27], v[156:159]\n\t"
    "v_cvt_pk_bf16_f32 v4, v0, v1\n\t"
    "v_cndmask_b32_e64 v237, 0, v241, vcc\n\t"
    "v_smfmac_f32_16x16x64_bf16 v[56:59], v[72:75], v[20:27], v93\n\t"
    "v_cndmask_b32_e64 v238, 0, v241, vcc\n\t"
    "v_mfma_f32_16x16x32_bf16 v[16:19], a[96:99], a[64:67], v[48:51]\n\t"
    "v_exp_f32_e32 v8, v8\n\t"
    "v_mfma_f32_16x16x32_bf16 v[16:19], a[100:103], a[68:71], v[16:19]\n\t"
    "s_add_u32 s56, s56, 0x4000\n\t"
    "s_and_b32 s56, s56, 0x3ffff\n\t"
    "v_mfma_f32_16x16x32_bf16 v[16:19], a[104:107], a[72:75], v[16:19]\n\t"
    "v_exp_f32_e32 v9, v9\n\t"
    "v_mfma_f32_16x16x32_bf16 v[16:19], a[108:111], a[76:79], v[16:19]\n\t"
    "v_cvt_pk_bf16_f32 v8, v8, v9\n\t"
    "s_add_u32 s57, s57, s60\n\t"
    "v_mfma_f32_16x16x32_bf16 v[20:23], a[112:115], a[64:67], v[48:51]\n\t"
    "v_exp_f32_e32 v10, v10\n\t"
    "v_mfma_f32_16x16x32_bf16 v[20:23], a[116:119], a[68:71], v[20:23]\n\t"
    "s_cmp_le_u32 s58, 12\n\t"
    "s_cselect_b32 s42, 0x40000, 0\n\t"
    "v_mfma_f32_16x16x32_bf16 v[20:23], a[120:123], a[72:75], v[20:23]\n\t"
    "v_exp_f32_e32 v11, v11\n\t"
    "v_mfma_f32_16x16x32_bf16 v[20:23], a[124:127], a[76:79], v[20:23]\n\t"
    "v_cvt_pk_bf16_f32 v9, v10, v11\n\t"
    "s_mov_b32 s46, s42\n\t"
    "v_mfma_f32_16x16x32_bf16 v[160:163], a[160:163], v[4:7], v[160:163]\n\t"
    "v_exp_f32_e32 v12, v12\n\t"
    "v_mfma_f32_16x16x32_bf16 v[164:167], a[164:167], v[4:7], v[164:167]\n\t"
    "s_mov_b32 m0, s57\n\t"
    "s_waitcnt lgkmcnt(0)\n\t"
    "v_mfma_f32_16x16x32_bf16 v[168:171], a[168:171], v[4:7], v[168:171]\n\t"
    "v_exp_f32_e32 v13, v13\n\t"
    "v_mfma_f32_16x16x32_bf16 v[172:175], a[172:175], v[4:7], v[172:175]\n\t"
    "v_cvt_pk_bf16_f32 v10, v12, v13\n\t"
    "s_add_u32 s58, s58, 1\n\t"
    "v_mfma_f32_16x16x32_bf16 v[176:179], a[176:179], v[4:7], v[176:179]\n\t"
    "v_exp_f32_e32 v14, v14\n\t"
    "v_mfma_f32_16x16x32_bf16 v[180:183], a[180:183], v[4:7], v[180:183]\n\t"
    "s_waitcnt vmcnt(13)\n\t"
    "s_barrier\n\t"
    "v_mfma_f32_16x16x32_bf16 v[184:187], a[184:187], v[4:7], v[184:187]\n\t"
    "v_exp_f32_e32 v15, v15\n\t"
    "v_mfma_f32_16x16x32_bf16 v[188:191], a[188:191], v[4:7], v[188:191]\n\t"
    "v_cvt_pk_bf16_f32 v11, v14, v15\n\t"
    "s_and_b32 s61, s58, 3\n\t"
    "v_mfma_f32_16x16x32_bf16 v[24:27], a[96:99], a[80:83], v[52:55]\n\t"
    "v_exp_f32_e32 v23, v23\n\t"
    "v_mfma_f32_16x16x32_bf16 v[24:27], a[100:103], a[84:87], v[24:27]\n\t"
    "ds_read_b128 a[96:99], v76 offset:0\n\t"
    "s_cmp_eq_u32 s61, 3\n\t"
    "v_mfma_f32_16x16x32_bf16 v[24:27], a[104:107], a[88:91], v[24:27]\n\t"
    "v_exp_f32_e32 v22, v22\n\t"
    "v_mfma_f32_16x16x32_bf16 v[24:27], a[108:111], a[92:95], v[24:27]\n\t"
    "ds_read_b128 a[100:103], v77 offset:0\n\t"
    "v_cvt_pk_bf16_f32 v23, v22, v23\n\t"
    "v_mfma_f32_16x16x32_bf16 v[28:31], a[112:115], a[80:83], v[52:55]\n\t"
    "v_exp_f32_e32 v21, v21\n\t"
    "v_mfma_f32_16x16x32_bf16 v[28:31], a[116:119], a[84:87], v[28:31]\n\t"
    "ds_read_b128 a[104:107], v78 offset:0\n\t"
    "ds_read_b128 a[108:111], v79 offset:0\n\t"
    "v_mfma_f32_16x16x32_bf16 v[28:31], a[120:123], a[88:91], v[28:31]\n\t"
    "v_exp_f32_e32 v20, v20\n\t"
    "v_mfma_f32_16x16x32_bf16 v[28:31], a[124:127], a[92:95], v[28:31]\n\t"
    "ds_read_b128 a[112:115], v76 offset:4096\n\t"
    "v_cvt_pk_bf16_f32 v22, v20, v21\n\t"
    "v_mfma_f32_16x16x32_bf16 a[192:195], a[160:163], v[8:11], a[192:195]\n\t"
    "v_exp_f32_e32 v19, v19\n\t"
    "v_mfma_f32_16x16x32_bf16 a[196:199], a[164:167], v[8:11], a[196:199]\n\t"
    "ds_read_b128 a[116:119], v77 offset:4096\n\t"
    "ds_read_b128 a[120:123], v78 offset:4096\n\t"
    "v_mfma_f32_16x16x32_bf16 a[200:203], a[168:171], v[8:11], a[200:203]\n\t"
    "v_exp_f32_e32 v18, v18\n\t"
    "v_mfma_f32_16x16x32_bf16 a[204:207], a[172:175], v[8:11], a[204:207]\n\t"
    "ds_read_b128 a[124:127], v79 offset:4096\n\t"
    "v_cvt_pk_bf16_f32 v21, v18, v19\n\t"
    "v_mfma_f32_16x16x32_bf16 a[208:211], a[176:179], v[8:11], a[208:211]\n\t"
    "v_exp_f32_e32 v17, v17\n\t"
    "v_mfma_f32_16x16x32_bf16 a[212:215], a[180:183], v[8:11], a[212:215]\n\t"
    "s_cselect_b32 s59, s63, 0x8000\n\t"
    "s_cmp_eq_u32 s61, 1\n\t"
    "v_mfma_f32_16x16x32_bf16 a[216:219], a[184:187], v[8:11], a[216:219]\n\t"
    "v_exp_f32_e32 v16, v16\n\t"
    "v_mfma_f32_16x16x32_bf16 a[220:223], a[188:191], v[8:11], a[220:223]\n\t"
    "v_cvt_pk_bf16_f32 v20, v16, v17\n\t"
    "s_cselect_b32 s60, s63, 0x8000\n\t"
    "v_smfmac_f32_16x16x64_bf16 v[60:63], v[72:75], v[4:11], v93\n\t"
    "s_waitcnt lgkmcnt(2)\n\t"
    "s_cmp_eq_u32 s58, 14\n\t"
    "v_mfma_f32_16x16x32_bf16 v[0:3], a[96:99], a[0:3], v[32:35]\n\t"
    "v_exp_f32_e32 v24, v24\n\t"
    "v_mfma_f32_16x16x32_bf16 v[0:3], a[100:103], a[4:7], v[0:3]\n\t"
    "s_waitcnt vmcnt(9)\n\t"
    "s_barrier\n\t"
    "v_mfma_f32_16x16x32_bf16 v[0:3], a[104:107], a[8:11], v[0:3]\n\t"
    "v_exp_f32_e32 v25, v25\n\t"
    "v_mfma_f32_16x16x32_bf16 v[0:3], a[108:111], a[12:15], v[0:3]\n\t"
    "ds_read_b64_tr_b16 a[128:129], v80 offset:0\n\t"
    "v_cvt_pk_bf16_f32 v24, v24, v25\n\t"
    "v_mfma_f32_16x16x32_bf16 v[4:7], a[112:115], a[0:3], v[32:35]\n\t"
    "v_exp_f32_e32 v26, v26\n\t"
    "v_mfma_f32_16x16x32_bf16 v[4:7], a[116:119], a[4:7], v[4:7]\n\t"
    "ds_read_b64_tr_b16 a[130:131], v80 offset:4096\n\t"
    "s_waitcnt lgkmcnt(2)\n\t"
    "v_mfma_f32_16x16x32_bf16 v[4:7], a[120:123], a[8:11], v[4:7]\n\t"
    "v_exp_f32_e32 v27, v27\n\t"
    "v_mfma_f32_16x16x32_bf16 v[4:7], a[124:127], a[12:15], v[4:7]\n\t"
    "ds_read_b64_tr_b16 a[132:133], v80 offset:8\n\t"
    "v_cvt_pk_bf16_f32 v25, v26, v27\n\t"
    "v_mfma_f32_16x16x32_bf16 a[224:227], a[160:163], v[20:23], a[224:227]\n\t"
    "v_exp_f32_e32 v28, v28\n\t"
    "v_mfma_f32_16x16x32_bf16 a[228:231], a[164:167], v[20:23], a[228:231]\n\t"
    "ds_read_b64_tr_b16 a[134:135], v80 offset:4104\n\t"
    "s_cselect_b32 s59, 0, s59\n\t"
    "v_mfma_f32_16x16x32_bf16 a[232:235], a[168:171], v[20:23], a[232:235]\n\t"
    "v_exp_f32_e32 v29, v29\n\t"
    "v_mfma_f32_16x16x32_bf16 a[236:239], a[172:175], v[20:23], a[236:239]\n\t"
    "ds_read_b64_tr_b16 a[136:137], v81 offset:0\n\t"
    "v_cvt_pk_bf16_f32 v26, v28, v29\n\t"
    "v_mfma_f32_16x16x32_bf16 a[240:243], a[176:179], v[20:23], a[240:243]\n\t"
    "v_exp_f32_e32 v30, v30\n\t"
    "v_mfma_f32_16x16x32_bf16 a[244:247], a[180:183], v[20:23], a[244:247]\n\t"
    "ds_read_b64_tr_b16 a[138:139], v81 offset:4096\n\t"
    "v_cndmask_b32_e64 v239, 0, v241, vcc\n\t"
    "v_mfma_f32_16x16x32_bf16 a[248:251], a[184:187], v[20:23], a[248:251]\n\t"
    "v_exp_f32_e32 v31, v31\n\t"
    "v_mfma_f32_16x16x32_bf16 a[252:255], a[188:191], v[20:23], a[252:255]\n\t"
    "ds_read_b64_tr_b16 a[140:141], v81 offset:8\n\t"
    "v_cvt_pk_bf16_f32 v27, v30, v31\n\t"
    "v_mfma_f32_16x16x32_bf16 v[8:11], a[96:99], a[16:19], v[36:39]\n\t"
    "v_exp_f32_e32 v7, v7\n\t"
    "v_mfma_f32_16x16x32_bf16 v[8:11], a[100:103], a[20:23], v[8:11]\n\t"
    "ds_read_b64_tr_b16 a[142:143], v81 offset:4104\n\t"
    "ds_read_b64_tr_b16 a[144:145], v82 offset:0\n\t"
    "v_mfma_f32_16x16x32_bf16 v[8:11], a[104:107], a[24:27], v[8:11]\n\t"
    "v_exp_f32_e32 v6, v6\n\t"
    "v_mfma_f32_16x16x32_bf16 v[8:11], a[108:111], a[28:31], v[8:11]\n\t"
    "ds_read_b64_tr_b16 a[146:147], v82 offset:4096\n\t"
    "v_cvt_pk_bf16_f32 v7, v6, v7\n\t"
    "v_mfma_f32_16x16x32_bf16 v[12:15], a[112:115], a[16:19], v[36:39]\n\t"
    "v_exp_f32_e32 v5, v5\n\t"
    "v_mfma_f32_16x16x32_bf16 v[12:15], a[116:119], a[20:23], v[12:15]\n\t"
    "ds_read_b64_tr_b16 a[148:149], v82 offset:8\n\t"
    "ds_read_b64_tr_b16 a[150:151], v82 offset:4104\n\t"
    "v_mfma_f32_16x16x32_bf16 v[12:15], a[120:123], a[24:27], v[12:15]\n\t"
    "v_exp_f32_e32 v4, v4\n\t"
    "v_mfma_f32_16x16x32_bf16 v[12:15], a[124:127], a[28:31], v[12:15]\n\t"
    "ds_read_b64_tr_b16 a[152:153], v83 offset:0\n\t"
    "v_cvt_pk_bf16_f32 v6, v4, v5\n\t"
    "v_mfma_f32_16x16x32_bf16 v[192:195], a[160:163], v[24:27], v[192:195]\n\t"
    "v_exp_f32_e32 v3, v3\n\t"
    "v_mfma_f32_16x16x32_bf16 v[196:199], a[164:167], v[24:27], v[196:199]\n\t"
    "ds_read_b64_tr_b16 a[154:155], v83 offset:4096\n\t"
    "ds_read_b64_tr_b16 a[156:157], v83 offset:8\n\t"
    "v_mfma_f32_16x16x32_bf16 v[200:203], a[168:171], v[24:27], v[200:203]\n\t"
    "v_exp_f32_e32 v2, v2\n\t"
    "v_mfma_f32_16x16x32_bf16 v[204:207], a[172:175], v[24:27], v[204:207]\n\t"
    "ds_read_b64_tr_b16 a[158:159], v83 offset:4104\n\t"
    "v_cvt_pk_bf16_f32 v5, v2, v3\n\t"
    "v_mfma_f32_16x16x32_bf16 v[208:211], a[176:179], v[24:27], v[208:211]\n\t"
    "v_exp_f32_e32 v1, v1\n\t"
    "v_mfma_f32_16x16x32_bf16 v[212:215], a[180:183], v[24:27], v[212:215]\n\t"
    "buffer_load_dwordx4 v88, s[40:43], s56 offen offset:0 nt lds\n\t"
    "v_mfma_f32_16x16x32_bf16 v[216:219], a[184:187], v[24:27], v[216:219]\n\t"
    "v_exp_f32_e32 v0, v0\n\t"
    "v_mfma_f32_16x16x32_bf16 v[220:223], a[188:191], v[24:27], v[220:223]\n\t"
    "v_cvt_pk_bf16_f32 v4, v0, v1\n\t"
    "s_mov_b64 s[72:73], 0\n\t"
    "v_smfmac_f32_16x16x64_bf16 v[64:67], v[72:75], v[20:27], v93\n\t"
    "buffer_load_dwordx4 v89, s[40:43], s56 offen offset:1024 nt lds\n\t"
    "v_mfma_f32_16x16x32_bf16 v[16:19], a[96:99], a[32:35], v[40:43]\n\t"
    "v_exp_f32_e32 v8, v8\n\t"
    "v_mfma_f32_16x16x32_bf16 v[16:19], a[100:103], a[36:39], v[16:19]\n\t"
    "buffer_load_dwordx4 v90, s[40:43], s56 offen offset:2048 nt lds\n\t"
    "v_mfma_f32_16x16x32_bf16 v[16:19], a[104:107], a[40:43], v[16:19]\n\t"
    "v_exp_f32_e32 v9, v9\n\t"
    "v_mfma_f32_16x16x32_bf16 v[16:19], a[108:111], a[44:47], v[16:19]\n\t"
    "v_cvt_pk_bf16_f32 v8, v8, v9\n\t"
    "s_waitcnt lgkmcnt(0)\n\t"
    "v_mfma_f32_16x16x32_bf16 v[20:23], a[112:115], a[32:35], v[40:43]\n\t"
    "v_exp_f32_e32 v10, v10\n\t"
    "v_mfma_f32_16x16x32_bf16 v[20:23], a[116:119], a[36:39], v[20:23]\n\t"
    "buffer_load_dwordx4 v91, s[40:43], s56 offen offset:3072 nt lds\n\t"
    "v_mfma_f32_16x16x32_bf16 v[20:23], a[120:123], a[40:43], v[20:23]\n\t"
    "v_exp_f32_e32 v11, v11\n\t"
    "v_mfma_f32_16x16x32_bf16 v[20:23], a[124:127], a[44:47], v[20:23]\n\t"
    "v_cvt_pk_bf16_f32 v9, v10, v11\n\t"
    "s_bitset1_b32 m0, 14\n\t"
    "v_mfma_f32_16x16x32_bf16 v[96:99], a[128:131], v[4:7], v[96:99]\n\t"
    "v_exp_f32_e32 v12, v12\n\t"
    "v_mfma_f32_16x16x32_bf16 v[100:103], a[132:135], v[4:7], v[100:103]\n\t"
    "ds_read_b64_tr_b16 a[160:161], v80 offset:8192\n\t"
    "v_add_u32_e32 v224, 0xffff0000, v224\n\t"
    "v_mfma_f32_16x16x32_bf16 v[104:107], a[136:139], v[4:7], v[104:107]\n\t"
    "v_exp_f32_e32 v13, v13\n\t"
    "v_mfma_f32_16x16x32_bf16 v[108:111], a[140:143], v[4:7], v[108:111]\n\t"
    "ds_read_b64_tr_b16 a[162:163], v80 offset:12288\n\t"
    "v_cvt_pk_bf16_f32 v10, v12, v13\n\t"
    "v_mfma_f32_16x16x32_bf16 v[112:115], a[144:147], v[4:7], v[112:115]\n\t"
    "v_exp_f32_e32 v14, v14\n\t"
    "v_mfma_f32_16x16x32_bf16 v[116:119], a[148:151], v[4:7], v[116:119]\n\t"
    "ds_read_b64_tr_b16 a[164:165], v80 offset:8200\n\t"
    "v_add_u32_e32 v225, 0xffff0000, v225\n\t"
    "v_mfma_f32_16x16x32_bf16 v[120:123], a[152:155], v[4:7], v[120:123]\n\t"
    "v_exp_f32_e32 v15, v15\n\t"
    "v_mfma_f32_16x16x32_bf16 v[124:127], a[156:159], v[4:7], v[124:127]\n\t"
    "ds_read_b64_tr_b16 a[166:167], v80 offset:12296\n\t"
    "v_cvt_pk_bf16_f32 v11, v14, v15\n\t"
    "v_mfma_f32_16x16x32_bf16 v[24:27], a[96:99], a[48:51], v[44:47]\n\t"
    "v_exp_f32_e32 v23, v23\n\t"
    "v_mfma_f32_16x16x32_bf16 v[24:27], a[100:103], a[52:55], v[24:27]\n\t"
    "ds_read_b64_tr_b16 a[168:169], v81 offset:8192\n\t"
    "v_add_u32_e32 v226, 0xffff0000, v226\n\t"
    "v_mfma_f32_16x16x32_bf16 v[24:27], a[104:107], a[56:59], v[24:27]\n\t"
    "v_exp_f32_e32 v22, v22\n\t"
    "v_mfma_f32_16x16x32_bf16 v[24:27], a[108:111], a[60:63], v[24:27]\n\t"
    "ds_read_b64_tr_b16 a[170:171], v81 offset:12288\n\t"
    "v_cvt_pk_bf16_f32 v23, v22, v23\n\t"
    "v_mfma_f32_16x16x32_bf16 v[28:31], a[112:115], a[48:51], v[44:47]\n\t"
    "v_exp_f32_e32 v21, v21\n\t"
    "v_mfma_f32_16x16x32_bf16 v[28:31], a[116:119], a[52:55], v[28:31]\n\t"
    "ds_read_b64_tr_b16 a[172:173], v81 offset:8200\n\t"
    "v_add_u32_e32 v227, 0xffff0000, v227\n\t"
    "v_mfma_f32_16x16x32_bf16 v[28:31], a[120:123], a[56:59], v[28:31]\n\t"
    "v_exp_f32_e32 v20, v20\n\t"
    "v_mfma_f32_16x16x32_bf16 v[28:31], a[124:127], a[60:63], v[28:31]\n\t"
    "ds_read_b64_tr_b16 a[174:175], v81 offset:12296\n\t"
    "v_cvt_pk_bf16_f32 v22, v20, v21\n\t"
    "v_mfma_f32_16x16x32_bf16 v[128:131], a[128:131], v[8:11], v[128:131]\n\t"
    "v_exp_f32_e32 v19, v19\n\t"
    "v_mfma_f32_16x16x32_bf16 v[132:135], a[132:135], v[8:11], v[132:135]\n\t"
    "ds_read_b64_tr_b16 a[176:177], v82 offset:8192\n\t"
    "v_add_u32_e32 v84, 0xffff0000, v84\n\t"
    "v_mfma_f32_16x16x32_bf16 v[136:139], a[136:139], v[8:11], v[136:139]\n\t"
    "v_exp_f32_e32 v18, v18\n\t"
    "v_mfma_f32_16x16x32_bf16 v[140:143], a[140:143], v[8:11], v[140:143]\n\t"
    "ds_read_b64_tr_b16 a[178:179], v82 offset:12288\n\t"
    "v_cvt_pk_bf16_f32 v21, v18, v19\n\t"
    "v_mfma_f32_16x16x32_bf16 v[144:147], a[144:147], v[8:11], v[144:147]\n\t"
    "v_exp_f32_e32 v17, v17\n\t"
    "v_mfma_f32_16x16x32_bf16 v[148:151], a[148:151], v[8:11], v[148:151]\n\t"
    "ds_read_b64_tr_b16 a[180:181], v82 offset:8200\n\t"
    "v_mfma_f32_16x16x32_bf16 v[152:155], a[152:155], v[8:11], v[152:155]\n\t"
    "v_exp_f32_e32 v16, v16\n\t"
    "v_mfma_f32_16x16x32_bf16 v[156:159], a[156:159], v[8:11], v[156:159]\n\t"
    "ds_read_b64_tr_b16 a[182:183], v82 offset:12296\n\t"
    "v_cvt_pk_bf16_f32 v20, v16, v17\n\t"
    "v_smfmac_f32_16x16x64_bf16 v[56:59], v[72:75], v[4:11], v93\n\t"
    "v_mfma_f32_16x16x32_bf16 v[0:3], a[96:99], a[64:67], v[48:51]\n\t"
    "v_exp_f32_e32 v24, v24\n\t"
    "v_mfma_f32_16x16x32_bf16 v[0:3], a[100:103], a[68:71], v[0:3]\n\t"
    "ds_read_b64_tr_b16 a[184:185], v83 offset:8192\n\t"
    "v_mfma_f32_16x16x32_bf16 v[0:3], a[104:107], a[72:75], v[0:3]\n\t"
    "v_exp_f32_e32 v25, v25\n\t"
    "v_mfma_f32_16x16x32_bf16 v[0:3], a[108:111], a[76:79], v[0:3]\n\t"
    "ds_read_b64_tr_b16 a[186:187], v83 offset:12288\n\t"
    "v_cvt_pk_bf16_f32 v24, v24, v25\n\t"
    "v_mfma_f32_16x16x32_bf16 v[4:7], a[112:115], a[64:67], v[48:51]\n\t"
    "v_exp_f32_e32 v26, v26\n\t"
    "v_mfma_f32_16x16x32_bf16 v[4:7], a[116:119], a[68:71], v[4:7]\n\t"
    "ds_read_b64_tr_b16 a[188:189], v83 offset:8200\n\t"
    "v_mfma_f32_16x16x32_bf16 v[4:7], a[120:123], a[72:75], v[4:7]\n\t"
    "v_exp_f32_e32 v27, v27\n\t"
    "v_mfma_f32_16x16x32_bf16 v[4:7], a[124:127], a[76:79], v[4:7]\n\t"
    "ds_read_b64_tr_b16 a[190:191], v83 offset:12296\n\t"
    "v_cvt_pk_bf16_f32 v25, v26, v27\n\t"
    "v_mfma_f32_16x16x32_bf16 v[160:163], a[128:131], v[20:23], v[160:163]\n\t"
    "v_exp_f32_e32 v28, v28\n\t"
    "v_mfma_f32_16x16x32_bf16 v[164:167], a[132:135], v[20:23], v[164:167]\n\t"
    "v_add_u32_e32 v80, s59, v80\n\t"
    "v_add_u32_e32 v81, s59, v81\n\t"
    "v_mfma_f32_16x16x32_bf16 v[168:171], a[136:139], v[20:23], v[168:171]\n\t"
    "v_exp_f32_e32 v29, v29\n\t"
    "v_mfma_f32_16x16x32_bf16 v[172:175], a[140:143], v[20:23], v[172:175]\n\t"
    "v_cvt_pk_bf16_f32 v26, v28, v29\n\t"
    "v_add_u32_e32 v82, s59, v82\n\t"
    "v_mfma_f32_16x16x32_bf16 v[176:179], a[144:147], v[20:23], v[176:179]\n\t"
    "v_exp_f32_e32 v30, v30\n\t"
    "v_mfma_f32_16x16x32_bf16 v[180:183], a[148:151], v[20:23], v[180:183]\n\t"
    "v_add_u32_e32 v83, s59, v83\n\t"
    "v_mfma_f32_16x16x32_bf16 v[184:187], a[152:155], v[20:23], v[184:187]\n\t"
    "v_exp_f32_e32 v31, v31\n\t"
    "v_mfma_f32_16x16x32_bf16 v[188:191], a[156:159], v[20:23], v[188:191]\n\t"
    "v_cvt_pk_bf16_f32 v27, v30, v31\n\t"
    "v_mfma_f32_16x16x32_bf16 v[8:11], a[96:99], a[80:83], v[52:55]\n\t"
    "v_exp_f32_e32 v7, v7\n\t"
    "v_mfma_f32_16x16x32_bf16 v[8:11], a[100:103], a[84:87], v[8:11]\n\t"
    "ds_read_b128 a[96:99], v76 offset:8192\n\t"
    "v_mfma_f32_16x16x32_bf16 v[8:11], a[104:107], a[88:91], v[8:11]\n\t"
    "v_exp_f32_e32 v6, v6\n\t"
    "v_mfma_f32_16x16x32_bf16 v[8:11], a[108:111], a[92:95], v[8:11]\n\t"
    "ds_read_b128 a[100:103], v77 offset:8192\n\t"
    "v_cvt_pk_bf16_f32 v7, v6, v7\n\t"
    "v_mfma_f32_16x16x32_bf16 v[12:15], a[112:115], a[80:83], v[52:55]\n\t"
    "v_exp_f32_e32 v5, v5\n\t"
    "v_mfma_f32_16x16x32_bf16 v[12:15], a[116:119], a[84:87], v[12:15]\n\t"
    "ds_read_b128 a[104:107], v78 offset:8192\n\t"
    "ds_read_b128 a[108:111], v79 offset:8192\n\t"
    "v_mfma_f32_16x16x32_bf16 v[12:15], a[120:123], a[88:91], v[12:15]\n\t"
    "v_exp_f32_e32 v4, v4\n\t"
    "v_mfma_f32_16x16x32_bf16 v[12:15], a[124:127], a[92:95], v[12:15]\n\t"
    "ds_read_b128 a[112:115], v76 offset:12288\n\t"
    "v_cvt_pk_bf16_f32 v6, v4, v5\n\t"
    "v_mfma_f32_16x16x32_bf16 a[192:195], a[128:131], v[24:27], a[192:195]\n\t"
    "v_exp_f32_e32 v3, v3\n\t"
    "v_mfma_f32_16x16x32_bf16 a[196:199], a[132:135], v[24:27], a[196:199]\n\t"
    "ds_read_b128 a[116:119], v77 offset:12288\n\t"
    "ds_read_b128 a[120:123], v78 offset:12288\n\t"
    "v_mfma_f32_16x16x32_bf16 a[200:203], a[136:139], v[24:27], a[200:203]\n\t"
    "v_exp_f32_e32 v2, v2\n\t"
    "v_mfma_f32_16x16x32_bf16 a[204:207], a[140:143], v[24:27], a[204:207]\n\t"
    "ds_read_b128 a[124:127], v79 offset:12288\n\t"
    "v_cvt_pk_bf16_f32 v5, v2, v3\n\t"
    "v_mfma_f32_16x16x32_bf16 a[208:211], a[144:147], v[24:27], a[208:211]\n\t"
    "v_exp_f32_e32 v1, v1\n\t"
    "v_mfma_f32_16x16x32_bf16 a[212:215], a[148:151], v[24:27], a[212:215]\n\t"
    "v_mfma_f32_16x16x32_bf16 a[216:219], a[152:155], v[24:27], a[216:219]\n\t"
    "v_exp_f32_e32 v0, v0\n\t"
    "v_mfma_f32_16x16x32_bf16 a[220:223], a[156:159], v[24:27], a[220:223]\n\t"
    "v_cvt_pk_bf16_f32 v4, v0, v1\n\t"
    "v_smfmac_f32_16x16x64_bf16 v[60:63], v[72:75], v[20:27], v93\n\t"
    "s_waitcnt lgkmcnt(2)\n\t"
    "v_add_u32_e32 v76, s59, v76\n\t"
    "v_mfma_f32_16x16x32_bf16 v[16:19], a[96:99], a[0:3], v[32:35]\n\t"
    "v_exp_f32_e32 v8, v8\n\t"
    "v_mfma_f32_16x16x32_bf16 v[16:19], a[100:103], a[4:7], v[16:19]\n\t"
    "buffer_load_dwordx4 v92, s[44:47], s56 offen offset:0 nt lds\n\t"
    "v_mfma_f32_16x16x32_bf16 v[16:19], a[104:107], a[8:11], v[16:19]\n\t"
    "v_exp_f32_e32 v9, v9\n\t"
    "v_mfma_f32_16x16x32_bf16 v[16:19], a[108:111], a[12:15], v[16:19]\n\t"
    "v_cvt_pk_bf16_f32 v8, v8, v9\n\t"
    "v_add_u32_e32 v77, s59, v77\n\t"
    "v_mfma_f32_16x16x32_bf16 v[20:23], a[112:115], a[0:3], v[32:35]\n\t"
    "v_exp_f32_e32 v10, v10\n\t"
    "v_mfma_f32_16x16x32_bf16 v[20:23], a[116:119], a[4:7], v[20:23]\n\t"
    "s_waitcnt lgkmcnt(0)\n\t"
    "v_add_u32_e32 v78, s59, v78\n\t"
    "v_mfma_f32_16x16x32_bf16 v[20:23], a[120:123], a[8:11], v[20:23]\n\t"
    "v_exp_f32_e32 v11, v11\n\t"
    "v_mfma_f32_16x16x32_bf16 v[20:23], a[124:127], a[12:15], v[20:23]\n\t"
    "v_cvt_pk_bf16_f32 v9, v10, v11\n\t"
    "v_add_u32_e32 v79, s59, v79\n\t"
    "v_mfma_f32_16x16x32_bf16 a[224:227], a[128:131], v[4:7], a[224:227]\n\t"
    "v_exp_f32_e32 v12, v12\n\t"
    "v_mfma_f32_16x16x32_bf16 a[228:231], a[132:135], v[4:7], a[228:231]\n\t"
    "buffer_load_dwordx4 v92, s[44:47], s56 offen offset:1024 nt lds\n\t"
    "v_mfma_f32_16x16x32_bf16 a[232:235], a[136:139], v[4:7], a[232:235]\n\t"
    "v_exp_f32_e32 v13, v13\n\t"
    "v_mfma_f32_16x16x32_bf16 a[236:239], a[140:143], v[4:7], a[236:239]\n\t"
    "buffer_load_dwordx4 v92, s[44:47], s56 offen offset:2048 nt lds\n\t"
    "v_cvt_pk_bf16_f32 v10, v12, v13\n\t"
    "v_mfma_f32_16x16x32_bf16 a[240:243], a[144:147], v[4:7], a[240:243]\n\t"
    "v_exp_f32_e32 v14, v14\n\t"
    "v_mfma_f32_16x16x32_bf16 a[244:247], a[148:151], v[4:7], a[244:247]\n\t"
    "buffer_load_dwordx4 v92, s[44:47], s56 offen offset:3072 nt lds\n\t"
    "s_waitcnt vmcnt(9)\n\t"
    "s_barrier\n\t"
    "v_mfma_f32_16x16x32_bf16 a[248:251], a[152:155], v[4:7], a[248:251]\n\t"
    "v_exp_f32_e32 v15, v15\n\t"
    "v_mfma_f32_16x16x32_bf16 a[252:255], a[156:159], v[4:7], a[252:255]\n\t"
    "v_cvt_pk_bf16_f32 v11, v14, v15\n\t"
    "s_cmp_eq_u32 %[wid], 3\n\t"
    "s_cbranch_scc1 LHE_%=\n\t"
    "LO_%=:\n\t"
    "v_mfma_f32_16x16x32_bf16 v[24:27], a[96:99], a[16:19], v[36:39]\n\t"
    "v_exp_f32_e32 v23, v23\n\t"
    "v_mfma_f32_16x16x32_bf16 v[24:27], a[100:103], a[20:23], v[24:27]\n\t"
    "s_add_u32 s56, s56, 0x4000\n\t"
    "s_and_b32 s56, s56, 0x3ffff\n\t"
    "v_mfma_f32_16x16x32_bf16 v[24:27], a[104:107], a[24:27], v[24:27]\n\t"
    "v_exp_f32_e32 v22, v22\n\t"
    "v_mfma_f32_16x16x32_bf16 v[24:27], a[108:111], a[28:31], v[24:27]\n\t"
    "v_cvt_pk_bf16_f32 v23, v22, v23\n\t"
    "s_add_u32 s57, s57, s60\n\t"
    "v_mfma_f32_16x16x32_bf16 v[28:31], a[112:115], a[16:19], v[36:39]\n\t"
    "v_exp_f32_e32 v21, v21\n\t"
    "v_mfma_f32_16x16x32_bf16 v[28:31], a[116:119], a[20:23], v[28:31]\n\t"
    "s_waitcnt lgkmcnt(0)\n\t"
    "s_cmp_le_u32 s58, 12\n\t"
    "v_mfma_f32_16x16x32_bf16 v[28:31], a[120:123], a[24:27], v[28:31]\n\t"
    "v_exp_f32_e32 v20, v20\n\t"
    "v_mfma_f32_16x16x32_bf16 v[28:31], a[124:127], a[28:31], v[28:31]\n\t"
    "v_cvt_pk_bf16_f32 v22, v20, v21\n\t"
    "s_cselect_b32 s42, 0x40000, 0\n\t"
    "v_mfma_f32_16x16x32_bf16 v[192:195], a[128:131], v[8:11], v[192:195]\n\t"
    "v_exp_f32_e32 v19, v19\n\t"
    "v_mfma_f32_16x16x32_bf16 v[196:199], a[132:135], v[8:11], v[196:199]\n\t"
    "s_mov_b32 s46, s42\n\t"
    "s_mov_b32 m0, s57\n\t"
    "v_mfma_f32_16x16x32_bf16 v[200:203], a[136:139], v[8:11], v[200:203]\n\t"
    "v_exp_f32_e32 v18, v18\n\t"
    "v_mfma_f32_16x16x32_bf16 v[204:207], a[140:143], v[8:11], v[204:207]\n\t"
    "ds_read_b64_tr_b16 a[128:129], v80 offset:0\n\t"
    "v_cvt_pk_bf16_f32 v21, v18, v19\n\t"
    "v_mfma_f32_16x16x32_bf16 v[208:211], a[144:147], v[8:11], v[208:211]\n\t"
    "v_exp_f32_e32 v17, v17\n\t"
    "v_mfma_f32_16x16x32_bf16 v[212:215], a[148:151], v[8:11], v[212:215]\n\t"
    "ds_read_b64_tr_b16 a[130:131], v80 offset:4096\n\t"
    "s_add_u32 s58, s58, 1\n\t"
    "v_mfma_f32_16x16x32_bf16 v[216:219], a[152:155], v[8:11], v[216:219]\n\t"
    "v_exp_f32_e32 v16, v16\n\t"
    "v_mfma_f32_16x16x32_bf16 v[220:223], a[156:159], v[8:11], v[220:223]\n\t"
    "ds_read_b64_tr_b16 a[132:133], v80 offset:8\n\t"
    "v_cvt_pk_bf16_f32 v20, v16, v17\n\t"
    "v_smfmac_f32_16x16x64_bf16 v[64:67], v[72:75], v[4:11], v93\n\t"
    "s_and_b32 s61, s58, 3\n\t"
    "s_cmp_eq_u32 s61, 3\n\t"
    "v_mfma_f32_16x16x32_bf16 v[0:3], a[96:99], a[32:35], v[40:43]\n\t"
    "v_exp_f32_e32 v24, v24\n\t"
    "v_mfma_f32_16x16x32_bf16 v[0:3], a[100:103], a[36:39], v[0:3]\n\t"
    "ds_read_b64_tr_b16 a[134:135], v80 offset:4104\n\t"
    "s_cselect_b32 s59, s63, 0x8000\n\t"
    "v_mfma_f32_16x16x32_bf16 v[0:3], a[104:107], a[40:43], v[0:3]\n\t"
    "v_exp_f32_e32 v25, v25\n\t"
    "v_mfma_f32_16x16x32_bf16 v[0:3], a[108:111], a[44:47], v[0:3]\n\t"
    "ds_read_b64_tr_b16 a[136:137], v81 offset:0\n\t"
    "v_cvt_pk_bf16_f32 v24, v24, v25\n\t"
    "v_mfma_f32_16x16x32_bf16 v[4:7], a[112:115], a[32:35], v[40:43]\n\t"
    "v_exp_f32_e32 v26, v26\n\t"
    "v_mfma_f32_16x16x32_bf16 v[4:7], a[116:119], a[36:39], v[4:7]\n\t"
    "ds_read_b64_tr_b16 a[138:139], v81 offset:4096\n\t"
    "s_cmp_eq_u32 s61, 1\n\t"
    "v_mfma_f32_16x16x32_bf16 v[4:7], a[120:123], a[40:43], v[4:7]\n\t"
    "v_exp_f32_e32 v27, v27\n\t"
    "v_mfma_f32_16x16x32_bf16 v[4:7], a[124:127], a[44:47], v[4:7]\n\t"
    "ds_read_b64_tr_b16 a[140:141], v81 offset:8\n\t"
    "v_cvt_pk_bf16_f32 v25, v26, v27\n\t"
    "v_mfma_f32_16x16x32_bf16 v[96:99], a[160:163], v[20:23], v[96:99]\n\t"
    "v_exp_f32_e32 v28, v28\n\t"
    "v_mfma_f32_16x16x32_bf16 v[100:103], a[164:167], v[20:23], v[100:103]\n\t"
    "ds_read_b64_tr_b16 a[142:143], v81 offset:4104\n\t"
    "s_cselect_b32 s60, s63, 0x8000\n\t"
    "v_mfma_f32_16x16x32_bf16 v[104:107], a[168:171], v[20:23], v[104:107]\n\t"
    "v_exp_f32_e32 v29, v29\n\t"
    "v_mfma_f32_16x16x32_bf16 v[108:111], a[172:175], v[20:23], v[108:111]\n\t"
    "ds_read_b64_tr_b16 a[144:145], v82 offset:0\n\t"
    "v_cvt_pk_bf16_f32 v26, v28, v29\n\t"
    "v_mfma_f32_16x16x32_bf16 v[112:115], a[176:179], v[20:23], v[112:115]\n\t"
    "v_exp_f32_e32 v30, v30\n\t"
    "v_mfma_f32_16x16x32_bf16 v[116:119], a[180:183], v[20:23], v[116:119]\n\t"
    "ds_read_b64_tr_b16 a[146:147], v82 offset:4096\n\t"
    "s_cmp_eq_u32 s58, 14\n\t"
    "v_mfma_f32_16x16x32_bf16 v[120:123], a[184:187], v[20:23], v[120:123]\n\t"
    "v_exp_f32_e32 v31, v31\n\t"
    "v_mfma_f32_16x16x32_bf16 v[124:127], a[188:191], v[20:23], v[124:127]\n\t"
    "ds_read_b64_tr_b16 a[148:149], v82 offset:8\n\t"
    "v_cvt_pk_bf16_f32 v27, v30, v31\n\t"
    "v_mfma_f32_16x16x32_bf16 v[8:11], a[96:99], a[48:51], v[44:47]\n\t"
    "v_exp_f32_e32 v7, v7\n\t"
    "v_mfma_f32_16x16x32_bf16 v[8:11], a[100:103], a[52:55], v[8:11]\n\t"
    "ds_read_b64_tr_b16 a[150:151], v82 offset:4104\n\t"
    "s_cselect_b32 s59, 0, s59\n\t"
    "v_mfma_f32_16x16x32_bf16 v[8:11], a[104:107], a[56:59], v[8:11]\n\t"
    "v_exp_f32_e32 v6, v6\n\t"
    "v_mfma_f32_16x16x32_bf16 v[8:11], a[108:111], a[60:63], v[8:11]\n\t"
    "ds_read_b64_tr_b16 a[152:153], v83 offset:0\n\t"
    "v_cvt_pk_bf16_f32 v7, v6, v7\n\t"
    "v_mfma_f32_16x16x32_bf16 v[12:15], a[112:115], a[48:51], v[44:47]\n\t"
    "v_exp_f32_e32 v5, v5\n\t"
    "v_mfma_f32_16x16x32_bf16 v[12:15], a[116:119], a[52:55], v[12:15]\n\t"
    "ds_read_b64_tr_b16 a[154:155], v83 offset:4096\n\t"
    "v_mfma_f32_16x16x32_bf16 v[12:15], a[120:123], a[56:59], v[12:15]\n\t"
    "v_exp_f32_e32 v4, v4\n\t"
    "v_mfma_f32_16x16x32_bf16 v[12:15], a[124:127], a[60:63], v[12:15]\n\t"
    "ds_read_b64_tr_b16 a[156:157], v83 offset:8\n\t"
    "v_cvt_pk_bf16_f32 v6, v4, v5\n\t"
    "v_mfma_f32_16x16x32_bf16 v[128:131], a[160:163], v[24:27], v[128:131]\n\t"
    "v_exp_f32_e32 v3, v3\n\t"
    "v_mfma_f32_16x16x32_bf16 v[132:135], a[164:167], v[24:27], v[132:135]\n\t"
    "ds_read_b64_tr_b16 a[158:159], v83 offset:4104\n\t"
    "v_mfma_f32_16x16x32_bf16 v[136:139], a[168:171], v[24:27], v[136:139]\n\t"
    "v_exp_f32_e32 v2, v2\n\t"
    "v_mfma_f32_16x16x32_bf16 v[140:143], a[172:175], v[24:27], v[140:143]\n\t"
    "v_cvt_pk_bf16_f32 v5, v2, v3\n\t"
    "v_mfma_f32_16x16x32_bf16 v[144:147], a[176:179], v[24:27], v[144:147]\n\t"
    "v_exp_f32_e32 v1, v1\n\t"
    "v_mfma_f32_16x16x32_bf16 v[148:151], a[180:183], v[24:27], v[148:151]\n\t"
    "v_mfma_f32_16x16x32_bf16 v[152:155], a[184:187], v[24:27], v[152:155]\n\t"
    "v_exp_f32_e32 v0, v0\n\t"
    "v_mfma_f32_16x16x32_bf16 v[156:159], a[188:191], v[24:27], v[156:159]\n\t"
    "v_cvt_pk_bf16_f32 v4, v0, v1\n\t"
    "v_smfmac_f32_16x16x64_bf16 v[56:59], v[72:75], v[20:27], v93\n\t"
    "v_mfma_f32_16x16x32_bf16 v[16:19], a[96:99], a[64:67], v[48:51]\n\t"
    "v_exp_f32_e32 v8, v8\n\t"
    "v_mfma_f32_16x16x32_bf16 v[16:19], a[100:103], a[68:71], v[16:19]\n\t"
    "buffer_load_dwordx4 v88, s[40:43], s56 offen offset:0 nt lds\n\t"
    "v_mfma_f32_16x16x32_bf16 v[16:19], a[104:107], a[72:75], v[16:19]\n\t"
    "v_exp_f32_e32 v9, v9\n\t"
    "v_mfma_f32_16x16x32_bf16 v[16:19], a[108:111], a[76:79], v[16:19]\n\t"
    "v_cvt_pk_bf16_f32 v8, v8, v9\n\t"
    "v_mfma_f32_16x16x32_bf16 v[20:23], a[112:115], a[64:67], v[48:51]\n\t"
    "v_exp_f32_e32 v10, v10\n\t"
    "v_mfma_f32_16x16x32_bf16 v[20:23], a[116:119], a[68:71], v[20:23]\n\t"
    "buffer_load_dwordx4 v89, s[40:43], s56 offen offset:1024 nt lds\n\t"
    "v_mfma_f32_16x16x32_bf16 v[20:23], a[120:123], a[72:75], v[20:23]\n\t"
    "v_exp_f32_e32 v11, v11\n\t"
    "v_mfma_f32_16x16x32_bf16 v[20:23], a[124:127], a[76:79], v[20:23]\n\t"
    "v_cvt_pk_bf16_f32 v9, v10, v11\n\t"
    "v_mfma_f32_16x16x32_bf16 v[160:163], a[160:163], v[4:7], v[160:163]\n\t"
    "v_exp_f32_e32 v12, v12\n\t"
    "v_mfma_f32_16x16x32_bf16 v[164:167], a[164:167], v[4:7], v[164:167]\n\t"
    "buffer_load_dwordx4 v90, s[40:43], s56 offen offset:2048 nt lds\n\t"
    "v_mfma_f32_16x16x32_bf16 v[168:171], a[168:171], v[4:7], v[168:171]\n\t"
    "v_exp_f32_e32 v13, v13\n\t"
    "v_mfma_f32_16x16x32_bf16 v[172:175], a[172:175], v[4:7], v[172:175]\n\t"
    "v_cvt_pk_bf16_f32 v10, v12, v13\n\t"
    "v_mfma_f32_16x16x32_bf16 v[176:179], a[176:179], v[4:7], v[176:179]\n\t"
    "v_exp_f32_e32 v14, v14\n\t"
    "v_mfma_f32_16x16x32_bf16 v[180:183], a[180:183], v[4:7], v[180:183]\n\t"
    "buffer_load_dwordx4 v91, s[40:43], s56 offen offset:3072 nt lds\n\t"
    "v_mfma_f32_16x16x32_bf16 v[184:187], a[184:187], v[4:7], v[184:187]\n\t"
    "v_exp_f32_e32 v15, v15\n\t"
    "v_mfma_f32_16x16x32_bf16 v[188:191], a[188:191], v[4:7], v[188:191]\n\t"
    "v_cvt_pk_bf16_f32 v11, v14, v15\n\t"
    "s_bitset1_b32 m0, 14\n\t"
    "v_mfma_f32_16x16x32_bf16 v[24:27], a[96:99], a[80:83], v[52:55]\n\t"
    "v_exp_f32_e32 v23, v23\n\t"
    "v_mfma_f32_16x16x32_bf16 v[24:27], a[100:103], a[84:87], v[24:27]\n\t"
    "ds_read_b128 a[96:99], v76 offset:0\n\t"
    "v_mfma_f32_16x16x32_bf16 v[24:27], a[104:107], a[88:91], v[24:27]\n\t"
    "v_exp_f32_e32 v22, v22\n\t"
    "v_mfma_f32_16x16x32_bf16 v[24:27], a[108:111], a[92:95], v[24:27]\n\t"
    "ds_read_b128 a[100:103], v77 offset:0\n\t"
    "v_cvt_pk_bf16_f32 v23, v22, v23\n\t"
    "v_mfma_f32_16x16x32_bf16 v[28:31], a[112:115], a[80:83], v[52:55]\n\t"
    "v_exp_f32_e32 v21, v21\n\t"
    "v_mfma_f32_16x16x32_bf16 v[28:31], a[116:119], a[84:87], v[28:31]\n\t"
    "ds_read_b128 a[104:107], v78 offset:0\n\t"
    "ds_read_b128 a[108:111], v79 offset:0\n\t"
    "v_mfma_f32_16x16x32_bf16 v[28:31], a[120:123], a[88:91], v[28:31]\n\t"
    "v_exp_f32_e32 v20, v20\n\t"
    "v_mfma_f32_16x16x32_bf16 v[28:31], a[124:127], a[92:95], v[28:31]\n\t"
    "ds_read_b128 a[112:115], v76 offset:4096\n\t"
    "v_cvt_pk_bf16_f32 v22, v20, v21\n\t"
    "v_mfma_f32_16x16x32_bf16 a[192:195], a[160:163], v[8:11], a[192:195]\n\t"
    "v_exp_f32_e32 v19, v19\n\t"
    "v_mfma_f32_16x16x32_bf16 a[196:199], a[164:167], v[8:11], a[196:199]\n\t"
    "ds_read_b128 a[116:119], v77 offset:4096\n\t"
    "ds_read_b128 a[120:123], v78 offset:4096\n\t"
    "v_mfma_f32_16x16x32_bf16 a[200:203], a[168:171], v[8:11], a[200:203]\n\t"
    "v_exp_f32_e32 v18, v18\n\t"
    "v_mfma_f32_16x16x32_bf16 a[204:207], a[172:175], v[8:11], a[204:207]\n\t"
    "ds_read_b128 a[124:127], v79 offset:4096\n\t"
    "v_cvt_pk_bf16_f32 v21, v18, v19\n\t"
    "v_mfma_f32_16x16x32_bf16 a[208:211], a[176:179], v[8:11], a[208:211]\n\t"
    "v_exp_f32_e32 v17, v17\n\t"
    "v_mfma_f32_16x16x32_bf16 a[212:215], a[180:183], v[8:11], a[212:215]\n\t"
    "v_mfma_f32_16x16x32_bf16 a[216:219], a[184:187], v[8:11], a[216:219]\n\t"
    "v_exp_f32_e32 v16, v16\n\t"
    "v_mfma_f32_16x16x32_bf16 a[220:223], a[188:191], v[8:11], a[220:223]\n\t"
    "v_cvt_pk_bf16_f32 v20, v16, v17\n\t"
    "v_smfmac_f32_16x16x64_bf16 v[60:63], v[72:75], v[4:11], v93\n\t"
    "s_waitcnt lgkmcnt(2)\n\t"
    "v_mfma_f32_16x16x32_bf16 v[0:3], a[96:99], a[0:3], v[32:35]\n\t"
    "v_exp_f32_e32 v24, v24\n\t"
    "v_mfma_f32_16x16x32_bf16 v[0:3], a[100:103], a[4:7], v[0:3]\n\t"
    "v_mfma_f32_16x16x32_bf16 v[0:3], a[104:107], a[8:11], v[0:3]\n\t"
    "v_exp_f32_e32 v25, v25\n\t"
    "v_mfma_f32_16x16x32_bf16 v[0:3], a[108:111], a[12:15], v[0:3]\n\t"
    "v_cvt_pk_bf16_f32 v24, v24, v25\n\t"
    "v_mfma_f32_16x16x32_bf16 v[4:7], a[112:115], a[0:3], v[32:35]\n\t"
    "v_exp_f32_e32 v26, v26\n\t"
    "v_mfma_f32_16x16x32_bf16 v[4:7], a[116:119], a[4:7], v[4:7]\n\t"
    "s_waitcnt lgkmcnt(0)\n\t"
    "v_mfma_f32_16x16x32_bf16 v[4:7], a[120:123], a[8:11], v[4:7]\n\t"
    "v_exp_f32_e32 v27, v27\n\t"
    "v_mfma_f32_16x16x32_bf16 v[4:7], a[124:127], a[12:15], v[4:7]\n\t"
    "v_cvt_pk_bf16_f32 v25, v26, v27\n\t"
    "v_mfma_f32_16x16x32_bf16 a[224:227], a[160:163], v[20:23], a[224:227]\n\t"
    "v_exp_f32_e32 v28, v28\n\t"
    "v_mfma_f32_16x16x32_bf16 a[228:231], a[164:167], v[20:23], a[228:231]\n\t"
    "v_mfma_f32_16x16x32_bf16 a[232:235], a[168:171], v[20:23], a[232:235]\n\t"
    "v_exp_f32_e32 v29, v29\n\t"
    "v_mfma_f32_16x16x32_bf16 a[236:239], a[172:175], v[20:23], a[236:239]\n\t"
    "v_cvt_pk_bf16_f32 v26, v28, v29\n\t"
    "v_mfma_f32_16x16x32_bf16 a[240:243], a[176:179], v[20:23], a[240:243]\n\t"
    "v_exp_f32_e32 v30, v30\n\t"
    "v_mfma_f32_16x16x32_bf16 a[244:247], a[180:183], v[20:23], a[244:247]\n\t"
    "v_mfma_f32_16x16x32_bf16 a[248:251], a[184:187], v[20:23], a[248:251]\n\t"
    "v_exp_f32_e32 v31, v31\n\t"
    "v_mfma_f32_16x16x32_bf16 a[252:255], a[188:191], v[20:23], a[252:255]\n\t"
    "v_cvt_pk_bf16_f32 v27, v30, v31\n\t"
    "v_mfma_f32_16x16x32_bf16 v[8:11], a[96:99], a[16:19], v[36:39]\n\t"
    "v_exp_f32_e32 v7, v7\n\t"
    "v_mfma_f32_16x16x32_bf16 v[8:11], a[100:103], a[20:23], v[8:11]\n\t"
    "v_mfma_f32_16x16x32_bf16 v[8:11], a[104:107], a[24:27], v[8:11]\n\t"
    "v_exp_f32_e32 v6, v6\n\t"
    "v_mfma_f32_16x16x32_bf16 v[8:11], a[108:111], a[28:31], v[8:11]\n\t"
    "v_cvt_pk_bf16_f32 v7, v6, v7\n\t"
    "v_mfma_f32_16x16x32_bf16 v[12:15], a[112:115], a[16:19], v[36:39]\n\t"
    "v_exp_f32_e32 v5, v5\n\t"
    "v_mfma_f32_16x16x32_bf16 v[12:15], a[116:119], a[20:23], v[12:15]\n\t"
    "v_mfma_f32_16x16x32_bf16 v[12:15], a[120:123], a[24:27], v[12:15]\n\t"
    "v_exp_f32_e32 v4, v4\n\t"
    "v_mfma_f32_16x16x32_bf16 v[12:15], a[124:127], a[28:31], v[12:15]\n\t"
    "v_cvt_pk_bf16_f32 v6, v4, v5\n\t"
    "v_mfma_f32_16x16x32_bf16 v[192:195], a[160:163], v[24:27], v[192:195]\n\t"
    "v_exp_f32_e32 v3, v3\n\t"
    "v_mfma_f32_16x16x32_bf16 v[196:199], a[164:167], v[24:27], v[196:199]\n\t"
    "ds_read_b64_tr_b16 a[160:161], v80 offset:8192\n\t"
    "v_mfma_f32_16x16x32_bf16 v[200:203], a[168:171], v[24:27], v[200:203]\n\t"
    "v_exp_f32_e32 v2, v2\n\t"
    "v_mfma_f32_16x16x32_bf16 v[204:207], a[172:175], v[24:27], v[204:207]\n\t"
    "ds_read_b64_tr_b16 a[162:163], v80 offset:12288\n\t"
    "v_cvt_pk_bf16_f32 v5, v2, v3\n\t"
    "v_mfma_f32_16x16x32_bf16 v[208:211], a[176:179], v[24:27], v[208:211]\n\t"
    "v_exp_f32_e32 v1, v1\n\t"
    "v_mfma_f32_16x16x32_bf16 v[212:215], a[180:183], v[24:27], v[212:215]\n\t"
    "ds_read_b64_tr_b16 a[164:165], v80 offset:8200\n\t"
    "v_mfma_f32_16x16x32_bf16 v[216:219], a[184:187], v[24:27], v[216:219]\n\t"
    "v_exp_f32_e32 v0, v0\n\t"
    "v_mfma_f32_16x16x32_bf16 v[220:223], a[188:191], v[24:27], v[220:223]\n\t"
    "ds_read_b64_tr_b16 a[166:167], v80 offset:12296\n\t"
    "v_cvt_pk_bf16_f32 v4, v0, v1\n\t"
    "v_smfmac_f32_16x16x64_bf16 v[64:67], v[72:75], v[20:27], v93\n\t"
    "v_mfma_f32_16x16x32_bf16 v[16:19], a[96:99], a[32:35], v[40:43]\n\t"
    "v_exp_f32_e32 v8, v8\n\t"
    "v_mfma_f32_16x16x32_bf16 v[16:19], a[100:103], a[36:39], v[16:19]\n\t"
    "ds_read_b64_tr_b16 a[168:169], v81 offset:8192\n\t"
    "v_mfma_f32_16x16x32_bf16 v[16:19], a[104:107], a[40:43], v[16:19]\n\t"
    "v_exp_f32_e32 v9, v9\n\t"
    "v_mfma_f32_16x16x32_bf16 v[16:19], a[108:111], a[44:47], v[16:19]\n\t"
    "ds_read_b64_tr_b16 a[170:171], v81 offset:12288\n\t"
    "v_cvt_pk_bf16_f32 v8, v8, v9\n\t"
    "v_mfma_f32_16x16x32_bf16 v[20:23], a[112:115], a[32:35], v[40:43]\n\t"
    "v_exp_f32_e32 v10, v10\n\t"
    "v_mfma_f32_16x16x32_bf16 v[20:23], a[116:119], a[36:39], v[20:23]\n\t"
    "ds_read_b64_tr_b16 a[172:173], v81 offset:8200\n\t"
    "v_mfma_f32_16x16x32_bf16 v[20:23], a[120:123], a[40:43], v[20:23]\n\t"
    "v_exp_f32_e32 v11, v11\n\t"
    "v_mfma_f32_16x16x32_bf16 v[20:23], a[124:127], a[44:47], v[20:23]\n\t"
    "ds_read_b64_tr_b16 a[174:175], v81 offset:12296\n\t"
    "v_cvt_pk_bf16_f32 v9, v10, v11\n\t"
    "v_mfma_f32_16x16x32_bf16 v[96:99], a[128:131], v[4:7], v[96:99]\n\t"
    "v_exp_f32_e32 v12, v12\n\t"
    "v_mfma_f32_16x16x32_bf16 v[100:103], a[132:135], v[4:7], v[100:103]\n\t"
    "ds_read_b64_tr_b16 a[176:177], v82 offset:8192\n\t"
    "v_mfma_f32_16x16x32_bf16 v[104:107], a[136:139], v[4:7], v[104:107]\n\t"
    "v_exp_f32_e32 v13, v13\n\t"
    "v_mfma_f32_16x16x32_bf16 v[108:111], a[140:143], v[4:7], v[108:111]\n\t"
    "ds_read_b64_tr_b16 a[178:179], v82 offset:12288\n\t"
    "v_cvt_pk_bf16_f32 v10, v12, v13\n\t"
    "v_mfma_f32_16x16x32_bf16 v[112:115], a[144:147], v[4:7], v[112:115]\n\t"
    "v_exp_f32_e32 v14, v14\n\t"
    "v_mfma_f32_16x16x32_bf16 v[116:119], a[148:151], v[4:7], v[116:119]\n\t"
    "ds_read_b64_tr_b16 a[180:181], v82 offset:8200\n\t"
    "v_mfma_f32_16x16x32_bf16 v[120:123], a[152:155], v[4:7], v[120:123]\n\t"
    "v_exp_f32_e32 v15, v15\n\t"
    "v_mfma_f32_16x16x32_bf16 v[124:127], a[156:159], v[4:7], v[124:127]\n\t"
    "ds_read_b64_tr_b16 a[182:183], v82 offset:12296\n\t"
    "v_cvt_pk_bf16_f32 v11, v14, v15\n\t"
    "v_mfma_f32_16x16x32_bf16 v[24:27], a[96:99], a[48:51], v[44:47]\n\t"
    "v_exp_f32_e32 v23, v23\n\t"
    "v_mfma_f32_16x16x32_bf16 v[24:27], a[100:103], a[52:55], v[24:27]\n\t"
    "ds_read_b64_tr_b16 a[184:185], v83 offset:8192\n\t"
    "v_mfma_f32_16x16x32_bf16 v[24:27], a[104:107], a[56:59], v[24:27]\n\t"
    "v_exp_f32_e32 v22, v22\n\t"
    "v_mfma_f32_16x16x32_bf16 v[24:27], a[108:111], a[60:63], v[24:27]\n\t"
    "ds_read_b64_tr_b16 a[186:187], v83 offset:12288\n\t"
    "v_cvt_pk_bf16_f32 v23, v22, v23\n\t"
    "v_mfma_f32_16x16x32_bf16 v[28:31], a[112:115], a[48:51], v[44:47]\n\t"
    "v_exp_f32_e32 v21, v21\n\t"
    "v_mfma_f32_16x16x32_bf16 v[28:31], a[116:119], a[52:55], v[28:31]\n\t"
    "ds_read_b64_tr_b16 a[188:189], v83 offset:8200\n\t"
    "v_mfma_f32_16x16x32_bf16 v[28:31], a[120:123], a[56:59], v[28:31]\n\t"
    "v_exp_f32_e32 v20, v20\n\t"
    "v_mfma_f32_16x16x32_bf16 v[28:31], a[124:127], a[60:63], v[28:31]\n\t"
    "ds_read_b64_tr_b16 a[190:191], v83 offset:12296\n\t"
    "v_cvt_pk_bf16_f32 v22, v20, v21\n\t"
    "v_mfma_f32_16x16x32_bf16 v[128:131], a[128:131], v[8:11], v[128:131]\n\t"
    "v_exp_f32_e32 v19, v19\n\t"
    "v_mfma_f32_16x16x32_bf16 v[132:135], a[132:135], v[8:11], v[132:135]\n\t"
    "v_mfma_f32_16x16x32_bf16 v[136:139], a[136:139], v[8:11], v[136:139]\n\t"
    "v_exp_f32_e32 v18, v18\n\t"
    "v_mfma_f32_16x16x32_bf16 v[140:143], a[140:143], v[8:11], v[140:143]\n\t"
    "v_cvt_pk_bf16_f32 v21, v18, v19\n\t"
    "v_mfma_f32_16x16x32_bf16 v[144:147], a[144:147], v[8:11], v[144:147]\n\t"
    "v_exp_f32_e32 v17, v17\n\t"
    "v_mfma_f32_16x16x32_bf16 v[148:151], a[148:151], v[8:11], v[148:151]\n\t"
    "v_exp_f32_e32 v24, v24\n\t"
    "v_mfma_f32_16x16x32_bf16 v[152:155], a[152:155], v[8:11], v[152:155]\n\t"
    "v_exp_f32_e32 v16, v16\n\t"
    "v_mfma_f32_16x16x32_bf16 v[156:159], a[156:159], v[8:11], v[156:159]\n\t"
    "v_cvt_pk_bf16_f32 v20, v16, v17\n\t"
    "v_smfmac_f32_16x16x64_bf16 v[56:59], v[72:75], v[4:11], v93\n\t"
    "v_mfma_f32_16x16x32_bf16 v[0:3], a[96:99], a[64:67], v[48:51]\n\t"
    "v_exp_f32_e32 v25, v25\n\t"
    "v_mfma_f32_16x16x32_bf16 v[0:3], a[100:103], a[68:71], v[0:3]\n\t"
    "ds_read_b128 a[96:99], v76 offset:8192\n\t"
    "v_cvt_pk_bf16_f32 v24, v24, v25\n\t"
    "v_mfma_f32_16x16x32_bf16 v[0:3], a[104:107], a[72:75], v[0:3]\n\t"
    "v_exp_f32_e32 v26, v26\n\t"
    "v_mfma_f32_16x16x32_bf16 v[0:3], a[108:111], a[76:79], v[0:3]\n\t"
    "ds_read_b128 a[100:103], v77 offset:8192\n\t"
    "v_mfma_f32_16x16x32_bf16 v[4:7], a[112:115], a[64:67], v[48:51]\n\t"
    "v_exp_f32_e32 v27, v27\n\t"
    "v_mfma_f32_16x16x32_bf16 v[4:7], a[116:119], a[68:71], v[4:7]\n\t"
    "ds_read_b128 a[104:107], v78 offset:8192\n\t"
    "v_cvt_pk_bf16_f32 v25, v26, v27\n\t"
    "v_mfma_f32_16x16x32_bf16 v[4:7], a[120:123], a[72:75], v[4:7]\n\t"
    "v_exp_f32_e32 v28, v28\n\t"
    "v_mfma_f32_16x16x32_bf16 v[4:7], a[124:127], a[76:79], v[4:7]\n\t"
    "ds_read_b128 a[108:111], v79 offset:8192\n\t"
    "v_mfma_f32_16x16x32_bf16 v[160:163], a[128:131], v[20:23], v[160:163]\n\t"
    "v_exp_f32_e32 v29, v29\n\t"
    "v_mfma_f32_16x16x32_bf16 v[164:167], a[132:135], v[20:23], v[164:167]\n\t"
    "ds_read_b128 a[112:115], v76 offset:12288\n\t"
    "v_cvt_pk_bf16_f32 v26, v28, v29\n\t"
    "v_mfma_f32_16x16x32_bf16 v[168:171], a[136:139], v[20:23], v[168:171]\n\t"
    "v_exp_f32_e32 v30, v30\n\t"
    "v_mfma_f32_16x16x32_bf16 v[172:175], a[140:143], v[20:23], v[172:175]\n\t"
    "ds_read_b128 a[116:119], v77 offset:12288\n\t"
    "v_mfma_f32_16x16x32_bf16 v[176:179], a[144:147], v[20:23], v[176:179]\n\t"
    "v_exp_f32_e32 v31, v31\n\t"
    "v_mfma_f32_16x16x32_bf16 v[180:183], a[148:151], v[20:23], v[180:183]\n\t"
    "ds_read_b128 a[120:123], v78 offset:12288\n\t"
    "v_cvt_pk_bf16_f32 v27, v30, v31\n\t"
    "v_mfma_f32_16x16x32_bf16 v[184:187], a[152:155], v[20:23], v[184:187]\n\t"
    "v_mfma_f32_16x16x32_bf16 v[188:191], a[156:159], v[20:23], v[188:191]\n\t"
    "ds_read_b128 a[124:127], v79 offset:12288\n\t"
    "v_mfma_f32_16x16x32_bf16 a[192:195], a[128:131], v[24:27], a[192:195]\n\t"
    "v_exp_f32_e32 v7, v7\n\t"
    "v_mfma_f32_16x16x32_bf16 a[196:199], a[132:135], v[24:27], a[196:199]\n\t"
    "v_exp_f32_e32 v6, v6\n\t"
    "v_mfma_f32_16x16x32_bf16 a[200:203], a[136:139], v[24:27], a[200:203]\n\t"
    "v_cvt_pk_bf16_f32 v7, v6, v7\n\t"
    "v_add_u32_e32 v76, s59, v76\n\t"
    "v_mfma_f32_16x16x32_bf16 a[204:207], a[140:143], v[24:27], a[204:207]\n\t"
    "v_exp_f32_e32 v5, v5\n\t"
    "v_mfma_f32_16x16x32_bf16 a[208:211], a[144:147], v[24:27], a[208:211]\n\t"
    "v_exp_f32_e32 v4, v4\n\t"
    "v_mfma_f32_16x16x32_bf16 a[212:215], a[148:151], v[24:27], a[212:215]\n\t"
    "v_cvt_pk_bf16_f32 v6, v4, v5\n\t"
    "v_add_u32_e32 v77, s59, v77\n\t"
    "v_mfma_f32_16x16x32_bf16 a[216:219], a[152:155], v[24:27], a[216:219]\n\t"
    "v_exp_f32_e32 v3, v3\n\t"
    "v_mfma_f32_16x16x32_bf16 a[220:223], a[156:159], v[24:27], a[220:223]\n\t"
    "v_add_u32_e32 v78, s59, v78\n\t"
    "v_add_u32_e32 v79, s59, v79\n\t"
    "v_smfmac_f32_16x16x64_bf16 v[60:63], v[72:75], v[20:27], v93\n\t"
    "s_waitcnt lgkmcnt(4)\n\t"
    "v_add_u32_e32 v80, s59, v80\n\t"
    "v_mfma_f32_16x16x32_bf16 v[16:19], a[96:99], a[0:3], v[32:35]\n\t"
    "v_exp_f32_e32 v2, v2\n\t"
    "v_mfma_f32_16x16x32_bf16 v[16:19], a[100:103], a[4:7], v[16:19]\n\t"
    "v_cvt_pk_bf16_f32 v5, v2, v3\n\t"
    "v_add_u32_e32 v81, s59, v81\n\t"
    "v_mfma_f32_16x16x32_bf16 v[16:19], a[104:107], a[8:11], v[16:19]\n\t"
    "v_exp_f32_e32 v1, v1\n\t"
    "v_mfma_f32_16x16x32_bf16 v[16:19], a[108:111], a[12:15], v[16:19]\n\t"
    "s_waitcnt lgkmcnt(0)\n\t"
    "v_add_u32_e32 v82, s59, v82\n\t"
    "v_mfma_f32_16x16x32_bf16 v[20:23], a[112:115], a[0:3], v[32:35]\n\t"
    "v_exp_f32_e32 v0, v0\n\t"
    "v_mfma_f32_16x16x32_bf16 v[20:23], a[116:119], a[4:7], v[20:23]\n\t"
    "v_cvt_pk_bf16_f32 v4, v0, v1\n\t"
    "v_add_u32_e32 v83, s59, v83\n\t"
    "v_mfma_f32_16x16x32_bf16 v[20:23], a[120:123], a[8:11], v[20:23]\n\t"
    "s_waitcnt vmcnt(4)\n\t"
    "s_barrier\n\t"
    "v_mfma_f32_16x16x32_bf16 v[20:23], a[124:127], a[12:15], v[20:23]\n\t"
    "ds_read_b128 v[8:11], v86 offset:0\n\t"
    "v_mfma_f32_16x16x32_bf16 a[224:227], a[128:131], v[4:7], a[224:227]\n\t"
    "buffer_load_dwordx4 v92, s[44:47], s56 offen offset:0 nt lds\n\t"
    "v_mfma_f32_16x16x32_bf16 a[228:231], a[132:135], v[4:7], a[228:231]\n\t"
    "v_xor_b32_e32 v86, 0x400, v86\n\t"
    "v_mfma_f32_16x16x32_bf16 a[232:235], a[136:139], v[4:7], a[232:235]\n\t"
    "buffer_load_dwordx4 v92, s[44:47], s56 offen offset:1024 nt lds\n\t"
    "v_mfma_f32_16x16x32_bf16 a[236:239], a[140:143], v[4:7], a[236:239]\n\t"
    "v_mfma_f32_16x16x32_bf16 a[240:243], a[144:147], v[4:7], a[240:243]\n\t"
    "buffer_load_dwordx4 v92, s[44:47], s56 offen offset:2048 nt lds\n\t"
    "v_mfma_f32_16x16x32_bf16 a[244:247], a[148:151], v[4:7], a[244:247]\n\t"
    "v_mfma_f32_16x16x32_bf16 a[248:251], a[152:155], v[4:7], a[248:251]\n\t"
    "buffer_load_dwordx4 v92, s[44:47], s56 offen offset:3072 nt lds\n\t"
    "v_mfma_f32_16x16x32_bf16 a[252:255], a[156:159], v[4:7], a[252:255]\n\t"
    "s_cbranch_scc0 LO_%=\n\t"
    "LT_%=:\n\t"
    "v_mfma_f32_16x16x32_bf16 v[24:27], a[96:99], a[16:19], v[36:39]\n\t"
    "v_exp_f32_e32 v23, v23\n\t"
    "v_mfma_f32_16x16x32_bf16 v[24:27], a[100:103], a[20:23], v[24:27]\n\t"
    "v_mfma_f32_16x16x32_bf16 v[24:27], a[104:107], a[24:27], v[24:27]\n\t"
    "v_exp_f32_e32 v22, v22\n\t"
    "v_mfma_f32_16x16x32_bf16 v[24:27], a[108:111], a[28:31], v[24:27]\n\t"
    "v_cvt_pk_bf16_f32 v23, v22, v23\n\t"
    "v_mfma_f32_16x16x32_bf16 v[28:31], a[112:115], a[16:19], v[36:39]\n\t"
    "v_exp_f32_e32 v21, v21\n\t"
    "v_mfma_f32_16x16x32_bf16 v[28:31], a[116:119], a[20:23], v[28:31]\n\t"
    "s_waitcnt lgkmcnt(0)\n\t"
    "v_mfma_f32_16x16x32_bf16 v[28:31], a[120:123], a[24:27], v[28:31]\n\t"
    "v_exp_f32_e32 v20, v20\n\t"
    "v_mfma_f32_16x16x32_bf16 v[28:31], a[124:127], a[28:31], v[28:31]\n\t"
    "v_cvt_pk_bf16_f32 v22, v20, v21\n\t"
    "v_mfma_f32_16x16x32_bf16 v[192:195], a[128:131], v[8:11], v[192:195]\n\t"
    "v_exp_f32_e32 v19, v19\n\t"
    "v_mfma_f32_16x16x32_bf16 v[196:199], a[132:135], v[8:11], v[196:199]\n\t"
    "ds_read_b64_tr_b16 a[128:129], v80 offset:32768\n\t"
    "v_mfma_f32_16x16x32_bf16 v[200:203], a[136:139], v[8:11], v[200:203]\n\t"
    "v_exp_f32_e32 v18, v18\n\t"
    "v_mfma_f32_16x16x32_bf16 v[204:207], a[140:143], v[8:11], v[204:207]\n\t"
    "ds_read_b64_tr_b16 a[130:131], v80 offset:36864\n\t"
    "v_cvt_pk_bf16_f32 v21, v18, v19\n\t"
    "v_mfma_f32_16x16x32_bf16 v[208:211], a[144:147], v[8:11], v[208:211]\n\t"
    "v_exp_f32_e32 v17, v17\n\t"
    "v_mfma_f32_16x16x32_bf16 v[212:215], a[148:151], v[8:11], v[212:215]\n\t"
    "ds_read_b64_tr_b16 a[132:133], v80 offset:32776\n\t"
    "v_mfma_f32_16x16x32_bf16 v[216:219], a[152:155], v[8:11], v[216:219]\n\t"
    "v_exp_f32_e32 v16, v16\n\t"
    "v_mfma_f32_16x16x32_bf16 v[220:223], a[156:159], v[8:11], v[220:223]\n\t"
    "ds_read_b64_tr_b16 a[134:135], v80 offset:36872\n\t"
    "v_cvt_pk_bf16_f32 v20, v16, v17\n\t"
    "v_smfmac_f32_16x16x64_bf16 v[64:67], v[72:75], v[4:11], v93\n\t"
    "v_mfma_f32_16x16x32_bf16 v[0:3], a[96:99], a[32:35], v[40:43]\n\t"
    "v_exp_f32_e32 v24, v24\n\t"
    "v_mfma_f32_16x16x32_bf16 v[0:3], a[100:103], a[36:39], v[0:3]\n\t"
    "ds_read_b128 a[96:99], v76 offset:32768\n\t"
    "v_mfma_f32_16x16x32_bf16 v[0:3], a[104:107], a[40:43], v[0:3]\n\t"
    "v_exp_f32_e32 v25, v25\n\t"
    "v_mfma_f32_16x16x32_bf16 v[0:3], a[108:111], a[44:47], v[0:3]\n\t"
    "ds_read_b128 a[100:103], v77 offset:32768\n\t"
    "v_cvt_pk_bf16_f32 v24, v24, v25\n\t"
    "v_mfma_f32_16x16x32_bf16 v[4:7], a[112:115], a[32:35], v[40:43]\n\t"
    "v_exp_f32_e32 v26, v26\n\t"
    "v_mfma_f32_16x16x32_bf16 v[4:7], a[116:119], a[36:39], v[4:7]\n\t"
    "ds_read_b128 a[104:107], v78 offset:32768\n\t"
    "ds_read_b128 a[108:111], v79 offset:32768\n\t"
    "v_mfma_f32_16x16x32_bf16 v[4:7], a[120:123], a[40:43], v[4:7]\n\t"
    "v_exp_f32_e32 v27, v27\n\t"
    "v_mfma_f32_16x16x32_bf16 v[4:7], a[124:127], a[44:47], v[4:7]\n\t"
    "ds_read_b128 a[112:115], v76 offset:36864\n\t"
    "v_cvt_pk_bf16_f32 v25, v26, v27\n\t"
    "v_mfma_f32_16x16x32_bf16 v[96:99], a[160:163], v[20:23], v[96:99]\n\t"
    "v_exp_f32_e32 v28, v28\n\t"
    "v_mfma_f32_16x16x32_bf16 v[100:103], a[164:167], v[20:23], v[100:103]\n\t"
    "ds_read_b128 a[116:119], v77 offset:36864\n\t"
    "ds_read_b128 a[120:123], v78 offset:36864\n\t"
    "v_mfma_f32_16x16x32_bf16 v[104:107], a[168:171], v[20:23], v[104:107]\n\t"
    "v_exp_f32_e32 v29, v29\n\t"
    "v_mfma_f32_16x16x32_bf16 v[108:111], a[172:175], v[20:23], v[108:111]\n\t"
    "ds_read_b128 a[124:127], v79 offset:36864\n\t"
    "v_cvt_pk_bf16_f32 v26, v28, v29\n\t"
    "v_mfma_f32_16x16x32_bf16 v[112:115], a[176:179], v[20:23], v[112:115]\n\t"
    "v_exp_f32_e32 v30, v30\n\t"
    "v_mfma_f32_16x16x32_bf16 v[116:119], a[180:183], v[20:23], v[116:119]\n\t"
    "v_mfma_f32_16x16x32_bf16 v[120:123], a[184:187], v[20:23], v[120:123]\n\t"
    "v_exp_f32_e32 v31, v31\n\t"
    "v_mfma_f32_16x16x32_bf16 v[124:127], a[188:191], v[20:23], v[124:127]\n\t"
    "v_cvt_pk_bf16_f32 v27, v30, v31\n\t"
    "v_mfma_f32_16x16x32_bf16 v[56:59], v[232:235], v[20:23], v[56:59]\n\t"
    "s_waitcnt lgkmcnt(2)\n\t"
    "v_mfma_f32_16x16x32_bf16 v[8:11], a[96:99], a[0:3], v[32:35]\n\t"
    "v_exp_f32_e32 v7, v7\n\t"
    "s_waitcnt vmcnt(0)\n\t"
    "v_mfma_f32_16x16x32_bf16 v[8:11], a[100:103], a[4:7], v[8:11]\n\t"
    "ds_read_b64_tr_b16 a[136:137], v81 offset:32768\n\t"
    "v_mfma_f32_16x16x32_bf16 v[8:11], a[104:107], a[8:11], v[8:11]\n\t"
    "v_exp_f32_e32 v6, v6\n\t"
    "v_mfma_f32_16x16x32_bf16 v[8:11], a[108:111], a[12:15], v[8:11]\n\t"
    "ds_read_b64_tr_b16 a[138:139], v81 offset:36864\n\t"
    "v_cvt_pk_bf16_f32 v7, v6, v7\n\t"
    "v_mfma_f32_16x16x32_bf16 v[12:15], a[112:115], a[0:3], v[32:35]\n\t"
    "v_exp_f32_e32 v5, v5\n\t"
    "v_mfma_f32_16x16x32_bf16 v[12:15], a[116:119], a[4:7], v[12:15]\n\t"
    "ds_read_b64_tr_b16 a[140:141], v81 offset:32776\n\t"
    "s_waitcnt lgkmcnt(3)\n\t"
    "v_mfma_f32_16x16x32_bf16 v[12:15], a[120:123], a[8:11], v[12:15]\n\t"
    "v_exp_f32_e32 v4, v4\n\t"
    "v_mfma_f32_16x16x32_bf16 v[12:15], a[124:127], a[12:15], v[12:15]\n\t"
    "ds_read_b64_tr_b16 a[142:143], v81 offset:36872\n\t"
    "v_cvt_pk_bf16_f32 v6, v4, v5\n\t"
    "v_mfma_f32_16x16x32_bf16 v[128:131], a[160:163], v[24:27], v[128:131]\n\t"
    "v_exp_f32_e32 v3, v3\n\t"
    "v_mfma_f32_16x16x32_bf16 v[132:135], a[164:167], v[24:27], v[132:135]\n\t"
    "ds_read_b64_tr_b16 a[144:145], v82 offset:32768\n\t"
    "v_mfma_f32_16x16x32_bf16 v[136:139], a[168:171], v[24:27], v[136:139]\n\t"
    "v_exp_f32_e32 v2, v2\n\t"
    "v_mfma_f32_16x16x32_bf16 v[140:143], a[172:175], v[24:27], v[140:143]\n\t"
    "ds_read_b64_tr_b16 a[146:147], v82 offset:36864\n\t"
    "v_cvt_pk_bf16_f32 v5, v2, v3\n\t"
    "v_mfma_f32_16x16x32_bf16 v[144:147], a[176:179], v[24:27], v[144:147]\n\t"
    "v_exp_f32_e32 v1, v1\n\t"
    "v_mfma_f32_16x16x32_bf16 v[148:151], a[180:183], v[24:27], v[148:151]\n\t"
    "ds_read_b64_tr_b16 a[148:149], v82 offset:32776\n\t"
    "v_mfma_f32_16x16x32_bf16 v[152:155], a[184:187], v[24:27], v[152:155]\n\t"
    "v_exp_f32_e32 v0, v0\n\t"
    "v_mfma_f32_16x16x32_bf16 v[156:159], a[188:191], v[24:27], v[156:159]\n\t"
    "ds_read_b64_tr_b16 a[150:151], v82 offset:36872\n\t"
    "v_cvt_pk_bf16_f32 v4, v0, v1\n\t"
    "v_mfma_f32_16x16x32_bf16 v[56:59], v[236:239], v[24:27], v[56:59]\n\t"
    "v_mfma_f32_16x16x32_bf16 v[16:19], a[96:99], a[16:19], v[36:39]\n\t"
    "v_exp_f32_e32 v8, v8\n\t"
    "v_mfma_f32_16x16x32_bf16 v[16:19], a[100:103], a[20:23], v[16:19]\n\t"
    "ds_read_b64_tr_b16 a[152:153], v83 offset:32768\n\t"
    "v_mfma_f32_16x16x32_bf16 v[16:19], a[104:107], a[24:27], v[16:19]\n\t"
    "v_exp_f32_e32 v9, v9\n\t"
    "v_mfma_f32_16x16x32_bf16 v[16:19], a[108:111], a[28:31], v[16:19]\n\t"
    "ds_read_b64_tr_b16 a[154:155], v83 offset:36864\n\t"
    "v_cvt_pk_bf16_f32 v8, v8, v9\n\t"
    "v_mfma_f32_16x16x32_bf16 v[20:23], a[112:115], a[16:19], v[36:39]\n\t"
    "v_exp_f32_e32 v10, v10\n\t"
    "v_mfma_f32_16x16x32_bf16 v[20:23], a[116:119], a[20:23], v[20:23]\n\t"
    "ds_read_b64_tr_b16 a[156:157], v83 offset:32776\n\t"
    "v_mfma_f32_16x16x32_bf16 v[20:23], a[120:123], a[24:27], v[20:23]\n\t"
    "v_exp_f32_e32 v11, v11\n\t"
    "v_mfma_f32_16x16x32_bf16 v[20:23], a[124:127], a[28:31], v[20:23]\n\t"
    "ds_read_b64_tr_b16 a[158:159], v83 offset:36872\n\t"
    "v_cvt_pk_bf16_f32 v9, v10, v11\n\t"
    "v_mfma_f32_16x16x32_bf16 v[160:163], a[160:163], v[4:7], v[160:163]\n\t"
    "v_exp_f32_e32 v12, v12\n\t"
    "v_mfma_f32_16x16x32_bf16 v[164:167], a[164:167], v[4:7], v[164:167]\n\t"
    "ds_read_b64_tr_b16 a[160:161], v80 offset:40960\n\t"
    "v_mfma_f32_16x16x32_bf16 v[168:171], a[168:171], v[4:7], v[168:171]\n\t"
    "v_exp_f32_e32 v13, v13\n\t"
    "v_mfma_f32_16x16x32_bf16 v[172:175], a[172:175], v[4:7], v[172:175]\n\t"
    "ds_read_b64_tr_b16 a[162:163], v80 offset:45056\n\t"
    "v_cvt_pk_bf16_f32 v10, v12, v13\n\t"
    "v_mfma_f32_16x16x32_bf16 v[176:179], a[176:179], v[4:7], v[176:179]\n\t"
    "v_exp_f32_e32 v14, v14\n\t"
    "v_mfma_f32_16x16x32_bf16 v[180:183], a[180:183], v[4:7], v[180:183]\n\t"
    "ds_read_b64_tr_b16 a[164:165], v80 offset:40968\n\t"
    "v_mfma_f32_16x16x32_bf16 v[184:187], a[184:187], v[4:7], v[184:187]\n\t"
    "v_exp_f32_e32 v15, v15\n\t"
    "v_mfma_f32_16x16x32_bf16 v[188:191], a[188:191], v[4:7], v[188:191]\n\t"
    "ds_read_b64_tr_b16 a[166:167], v80 offset:45064\n\t"
    "v_cvt_pk_bf16_f32 v11, v14, v15\n\t"
    "v_mfma_f32_16x16x32_bf16 v[60:63], v[232:235], v[4:7], v[60:63]\n\t"
    "v_mfma_f32_16x16x32_bf16 v[24:27], a[96:99], a[32:35], v[40:43]\n\t"
    "v_exp_f32_e32 v23, v23\n\t"
    "v_mfma_f32_16x16x32_bf16 v[24:27], a[100:103], a[36:39], v[24:27]\n\t"
    "ds_read_b128 a[96:99], v76 offset:40960\n\t"
    "s_waitcnt lgkmcnt(5)\n\t"
    "v_mfma_f32_16x16x32_bf16 v[24:27], a[104:107], a[40:43], v[24:27]\n\t"
    "v_exp_f32_e32 v22, v22\n\t"
    "v_mfma_f32_16x16x32_bf16 v[24:27], a[108:111], a[44:47], v[24:27]\n\t"
    "ds_read_b128 a[100:103], v77 offset:40960\n\t"
    "v_cvt_pk_bf16_f32 v23, v22, v23\n\t"
    "v_mfma_f32_16x16x32_bf16 v[28:31], a[112:115], a[32:35], v[40:43]\n\t"
    "v_exp_f32_e32 v21, v21\n\t"
    "v_mfma_f32_16x16x32_bf16 v[28:31], a[116:119], a[36:39], v[28:31]\n\t"
    "ds_read_b128 a[104:107], v78 offset:40960\n\t"
    "ds_read_b128 a[108:111], v79 offset:40960\n\t"
    "v_mfma_f32_16x16x32_bf16 v[28:31], a[120:123], a[40:43], v[28:31]\n\t"
    "v_exp_f32_e32 v20, v20\n\t"
    "v_mfma_f32_16x16x32_bf16 v[28:31], a[124:127], a[44:47], v[28:31]\n\t"
    "ds_read_b128 a[112:115], v76 offset:45056\n\t"
    "v_cvt_pk_bf16_f32 v22, v20, v21\n\t"
    "v_mfma_f32_16x16x32_bf16 v[96:99], a[128:131], v[8:11], v[96:99]\n\t"
    "v_exp_f32_e32 v19, v19\n\t"
    "v_mfma_f32_16x16x32_bf16 v[100:103], a[132:135], v[8:11], v[100:103]\n\t"
    "ds_read_b128 a[116:119], v77 offset:45056\n\t"
    "ds_read_b128 a[120:123], v78 offset:45056\n\t"
    "v_mfma_f32_16x16x32_bf16 v[104:107], a[136:139], v[8:11], v[104:107]\n\t"
    "v_exp_f32_e32 v18, v18\n\t"
    "v_mfma_f32_16x16x32_bf16 v[108:111], a[140:143], v[8:11], v[108:111]\n\t"
    "ds_read_b128 a[124:127], v79 offset:45056\n\t"
    "v_cvt_pk_bf16_f32 v21, v18, v19\n\t"
    "v_mfma_f32_16x16x32_bf16 v[112:115], a[144:147], v[8:11], v[112:115]\n\t"
    "v_exp_f32_e32 v17, v17\n\t"
    "v_mfma_f32_16x16x32_bf16 v[116:119], a[148:151], v[8:11], v[116:119]\n\t"
    "v_mfma_f32_16x16x32_bf16 v[120:123], a[152:155], v[8:11], v[120:123]\n\t"
    "v_exp_f32_e32 v16, v16\n\t"
    "v_mfma_f32_16x16x32_bf16 v[124:127], a[156:159], v[8:11], v[124:127]\n\t"
    "v_cvt_pk_bf16_f32 v20, v16, v17\n\t"
    "v_mfma_f32_16x16x32_bf16 v[56:59], v[232:235], v[8:11], v[56:59]\n\t"
    "s_waitcnt lgkmcnt(2)\n\t"
    "v_mfma_f32_16x16x32_bf16 v[0:3], a[96:99], a[0:3], v[32:35]\n\t"
    "v_exp_f32_e32 v24, v24\n\t"
    "v_mfma_f32_16x16x32_bf16 v[0:3], a[100:103], a[4:7], v[0:3]\n\t"
    "ds_read_b64_tr_b16 a[168:169], v81 offset:40960\n\t"
    "v_mfma_f32_16x16x32_bf16 v[0:3], a[104:107], a[8:11], v[0:3]\n\t"
    "v_exp_f32_e32 v25, v25\n\t"
    "v_mfma_f32_16x16x32_bf16 v[0:3], a[108:111], a[12:15], v[0:3]\n\t"
    "ds_read_b64_tr_b16 a[170:171], v81 offset:45056\n\t"
    "v_cvt_pk_bf16_f32 v24, v24, v25\n\t"
    "v_mfma_f32_16x16x32_bf16 v[4:7], a[112:115], a[0:3], v[32:35]\n\t"
    "v_exp_f32_e32 v26, v26\n\t"
    "v_mfma_f32_16x16x32_bf16 v[4:7], a[116:119], a[4:7], v[4:7]\n\t"
    "ds_read_b64_tr_b16 a[172:173], v81 offset:40968\n\t"
    "s_waitcnt lgkmcnt(3)\n\t"
    "v_mfma_f32_16x16x32_bf16 v[4:7], a[120:123], a[8:11], v[4:7]\n\t"
    "v_exp_f32_e32 v27, v27\n\t"
    "v_mfma_f32_16x16x32_bf16 v[4:7], a[124:127], a[12:15], v[4:7]\n\t"
    "ds_read_b64_tr_b16 a[174:175], v81 offset:45064\n\t"
    "v_cvt_pk_bf16_f32 v25, v26, v27\n\t"
    "v_mfma_f32_16x16x32_bf16 v[128:131], a[128:131], v[20:23], v[128:131]\n\t"
    "v_exp_f32_e32 v28, v28\n\t"
    "v_mfma_f32_16x16x32_bf16 v[132:135], a[132:135], v[20:23], v[132:135]\n\t"
    "ds_read_b64_tr_b16 a[176:177], v82 offset:40960\n\t"
    "v_mfma_f32_16x16x32_bf16 v[136:139], a[136:139], v[20:23], v[136:139]\n\t"
    "v_exp_f32_e32 v29, v29\n\t"
    "v_mfma_f32_16x16x32_bf16 v[140:143], a[140:143], v[20:23], v[140:143]\n\t"
    "ds_read_b64_tr_b16 a[178:179], v82 offset:45056\n\t"
    "v_cvt_pk_bf16_f32 v26, v28, v29\n\t"
    "v_mfma_f32_16x16x32_bf16 v[144:147], a[144:147], v[20:23], v[144:147]\n\t"
    "v_exp_f32_e32 v30, v30\n\t"
    "v_mfma_f32_16x16x32_bf16 v[148:151], a[148:151], v[20:23], v[148:151]\n\t"
    "ds_read_b64_tr_b16 a[180:181], v82 offset:40968\n\t"
    "v_mfma_f32_16x16x32_bf16 v[152:155], a[152:155], v[20:23], v[152:155]\n\t"
    "v_exp_f32_e32 v31, v31\n\t"
    "v_mfma_f32_16x16x32_bf16 v[156:159], a[156:159], v[20:23], v[156:159]\n\t"
    "ds_read_b64_tr_b16 a[182:183], v82 offset:45064\n\t"
    "v_cvt_pk_bf16_f32 v27, v30, v31\n\t"
    "v_mfma_f32_16x16x32_bf16 v[56:59], v[236:239], v[20:23], v[56:59]\n\t"
    "v_mfma_f32_16x16x32_bf16 v[8:11], a[96:99], a[16:19], v[36:39]\n\t"
    "v_exp_f32_e32 v7, v7\n\t"
    "v_mfma_f32_16x16x32_bf16 v[8:11], a[100:103], a[20:23], v[8:11]\n\t"
    "ds_read_b64_tr_b16 a[184:185], v83 offset:40960\n\t"
    "v_mfma_f32_16x16x32_bf16 v[8:11], a[104:107], a[24:27], v[8:11]\n\t"
    "v_exp_f32_e32 v6, v6\n\t"
    "v_mfma_f32_16x16x32_bf16 v[8:11], a[108:111], a[28:31], v[8:11]\n\t"
    "ds_read_b64_tr_b16 a[186:187], v83 offset:45056\n\t"
    "v_cvt_pk_bf16_f32 v7, v6, v7\n\t"
    "v_mfma_f32_16x16x32_bf16 v[12:15], a[112:115], a[16:19], v[36:39]\n\t"
    "v_exp_f32_e32 v5, v5\n\t"
    "v_mfma_f32_16x16x32_bf16 v[12:15], a[116:119], a[20:23], v[12:15]\n\t"
    "ds_read_b64_tr_b16 a[188:189], v83 offset:40968\n\t"
    "v_mfma_f32_16x16x32_bf16 v[12:15], a[120:123], a[24:27], v[12:15]\n\t"
    "v_exp_f32_e32 v4, v4\n\t"
    "v_mfma_f32_16x16x32_bf16 v[12:15], a[124:127], a[28:31], v[12:15]\n\t"
    "ds_read_b64_tr_b16 a[190:191], v83 offset:45064\n\t"
    "v_cvt_pk_bf16_f32 v6, v4, v5\n\t"
    "v_mfma_f32_16x16x32_bf16 v[160:163], a[128:131], v[24:27], v[160:163]\n\t"
    "v_exp_f32_e32 v3, v3\n\t"
    "v_mfma_f32_16x16x32_bf16 v[164:167], a[132:135], v[24:27], v[164:167]\n\t"
    "ds_read_b64_tr_b16 a[128:129], v80 offset:8192\n\t"
    "v_mfma_f32_16x16x32_bf16 v[168:171], a[136:139], v[24:27], v[168:171]\n\t"
    "v_exp_f32_e32 v2, v2\n\t"
    "v_mfma_f32_16x16x32_bf16 v[172:175], a[140:143], v[24:27], v[172:175]\n\t"
    "ds_read_b64_tr_b16 a[130:131], v80 offset:12288\n\t"
    "v_cvt_pk_bf16_f32 v5, v2, v3\n\t"
    "v_mfma_f32_16x16x32_bf16 v[176:179], a[144:147], v[24:27], v[176:179]\n\t"
    "v_exp_f32_e32 v1, v1\n\t"
    "v_mfma_f32_16x16x32_bf16 v[180:183], a[148:151], v[24:27], v[180:183]\n\t"
    "ds_read_b64_tr_b16 a[132:133], v80 offset:8200\n\t"
    "v_mfma_f32_16x16x32_bf16 v[184:187], a[152:155], v[24:27], v[184:187]\n\t"
    "v_exp_f32_e32 v0, v0\n\t"
    "v_mfma_f32_16x16x32_bf16 v[188:191], a[156:159], v[24:27], v[188:191]\n\t"
    "ds_read_b64_tr_b16 a[134:135], v80 offset:12296\n\t"
    "v_cvt_pk_bf16_f32 v4, v0, v1\n\t"
    "v_mfma_f32_16x16x32_bf16 v[60:63], v[232:235], v[24:27], v[60:63]\n\t"
    "v_mfma_f32_16x16x32_bf16 v[16:19], a[96:99], a[32:35], v[40:43]\n\t"
    "v_exp_f32_e32 v8, v8\n\t"
    "v_mfma_f32_16x16x32_bf16 v[16:19], a[100:103], a[36:39], v[16:19]\n\t"
    "ds_read_b128 a[96:99], v76 offset:8192\n\t"
    "s_waitcnt lgkmcnt(5)\n\t"
    "v_mfma_f32_16x16x32_bf16 v[16:19], a[104:107], a[40:43], v[16:19]\n\t"
    "v_exp_f32_e32 v9, v9\n\t"
    "v_mfma_f32_16x16x32_bf16 v[16:19], a[108:111], a[44:47], v[16:19]\n\t"
    "ds_read_b128 a[100:103], v77 offset:8192\n\t"
    "v_cvt_pk_bf16_f32 v8, v8, v9\n\t"
    "v_mfma_f32_16x16x32_bf16 v[20:23], a[112:115], a[32:35], v[40:43]\n\t"
    "v_exp_f32_e32 v10, v10\n\t"
    "v_mfma_f32_16x16x32_bf16 v[20:23], a[116:119], a[36:39], v[20:23]\n\t"
    "ds_read_b128 a[104:107], v78 offset:8192\n\t"
    "ds_read_b128 a[108:111], v79 offset:8192\n\t"
    "v_mfma_f32_16x16x32_bf16 v[20:23], a[120:123], a[40:43], v[20:23]\n\t"
    "v_exp_f32_e32 v11, v11\n\t"
    "v_mfma_f32_16x16x32_bf16 v[20:23], a[124:127], a[44:47], v[20:23]\n\t"
    "ds_read_b128 a[112:115], v76 offset:12288\n\t"
    "v_cvt_pk_bf16_f32 v9, v10, v11\n\t"
    "v_mfma_f32_16x16x32_bf16 v[96:99], a[160:163], v[4:7], v[96:99]\n\t"
    "v_exp_f32_e32 v12, v12\n\t"
    "v_mfma_f32_16x16x32_bf16 v[100:103], a[164:167], v[4:7], v[100:103]\n\t"
    "ds_read_b128 a[116:119], v77 offset:12288\n\t"
    "ds_read_b128 a[120:123], v78 offset:12288\n\t"
    "v_mfma_f32_16x16x32_bf16 v[104:107], a[168:171], v[4:7], v[104:107]\n\t"
    "v_exp_f32_e32 v13, v13\n\t"
    "v_mfma_f32_16x16x32_bf16 v[108:111], a[172:175], v[4:7], v[108:111]\n\t"
    "ds_read_b128 a[124:127], v79 offset:12288\n\t"
    "v_cvt_pk_bf16_f32 v10, v12, v13\n\t"
    "v_mfma_f32_16x16x32_bf16 v[112:115], a[176:179], v[4:7], v[112:115]\n\t"
    "v_exp_f32_e32 v14, v14\n\t"
    "v_mfma_f32_16x16x32_bf16 v[116:119], a[180:183], v[4:7], v[116:119]\n\t"
    "v_mfma_f32_16x16x32_bf16 v[120:123], a[184:187], v[4:7], v[120:123]\n\t"
    "v_exp_f32_e32 v15, v15\n\t"
    "v_mfma_f32_16x16x32_bf16 v[124:127], a[188:191], v[4:7], v[124:127]\n\t"
    "v_cvt_pk_bf16_f32 v11, v14, v15\n\t"
    "v_mfma_f32_16x16x32_bf16 v[56:59], v[232:235], v[4:7], v[56:59]\n\t"
    "s_waitcnt lgkmcnt(2)\n\t"
    "v_mfma_f32_16x16x32_bf16 v[24:27], a[96:99], a[48:51], v[44:47]\n\t"
    "v_exp_f32_e32 v23, v23\n\t"
    "v_mfma_f32_16x16x32_bf16 v[24:27], a[100:103], a[52:55], v[24:27]\n\t"
    "ds_read_b64_tr_b16 a[136:137], v81 offset:8192\n\t"
    "v_mfma_f32_16x16x32_bf16 v[24:27], a[104:107], a[56:59], v[24:27]\n\t"
    "v_exp_f32_e32 v22, v22\n\t"
    "v_mfma_f32_16x16x32_bf16 v[24:27], a[108:111], a[60:63], v[24:27]\n\t"
    "ds_read_b64_tr_b16 a[138:139], v81 offset:12288\n\t"
    "v_cvt_pk_bf16_f32 v23, v22, v23\n\t"
    "v_mfma_f32_16x16x32_bf16 v[28:31], a[112:115], a[48:51], v[44:47]\n\t"
    "v_exp_f32_e32 v21, v21\n\t"
    "v_mfma_f32_16x16x32_bf16 v[28:31], a[116:119], a[52:55], v[28:31]\n\t"
    "ds_read_b64_tr_b16 a[140:141], v81 offset:8200\n\t"
    "s_waitcnt lgkmcnt(3)\n\t"
    "v_mfma_f32_16x16x32_bf16 v[28:31], a[120:123], a[56:59], v[28:31]\n\t"
    "v_exp_f32_e32 v20, v20\n\t"
    "v_mfma_f32_16x16x32_bf16 v[28:31], a[124:127], a[60:63], v[28:31]\n\t"
    "ds_read_b64_tr_b16 a[142:143], v81 offset:12296\n\t"
    "v_cvt_pk_bf16_f32 v22, v20, v21\n\t"
    "v_add_f32_e32 v56, v57, v56\n\t"
    "v_cmp_ngt_f32_e32 vcc, s70, v56\n\t"
    "v_rcp_f32_e32 v56, v56\n\t"
    "s_or_b64 s[72:73], s[72:73], vcc\n\t"
    "v_pk_mul_f32 v[96:97], v[96:97], v[56:57] op_sel_hi:[1,0]\n\t"
    "v_pk_mul_f32 v[98:99], v[98:99], v[56:57] op_sel_hi:[1,0]\n\t"
    "v_pk_mul_f32 v[100:101], v[100:101], v[56:57] op_sel_hi:[1,0]\n\t"
    "v_pk_mul_f32 v[102:103], v[102:103], v[56:57] op_sel_hi:[1,0]\n\t"
    "v_cvt_pk_bf16_f32 v96, v96, v97\n\t"
    "v_cvt_pk_bf16_f32 v97, v98, v99\n\t"
    "v_cvt_pk_bf16_f32 v98, v100, v101\n\t"
    "v_cvt_pk_bf16_f32 v99, v102, v103\n\t"
    "v_pk_mul_f32 v[104:105], v[104:105], v[56:57] op_sel_hi:[1,0]\n\t"
    "v_pk_mul_f32 v[106:107], v[106:107], v[56:57] op_sel_hi:[1,0]\n\t"
    "v_pk_mul_f32 v[108:109], v[108:109], v[56:57] op_sel_hi:[1,0]\n\t"
    "v_pk_mul_f32 v[110:111], v[110:111], v[56:57] op_sel_hi:[1,0]\n\t"
    "v_cvt_pk_bf16_f32 v104, v104, v105\n\t"
    "v_cvt_pk_bf16_f32 v105, v106, v107\n\t"
    "v_cvt_pk_bf16_f32 v106, v108, v109\n\t"
    "v_cvt_pk_bf16_f32 v107, v110, v111\n\t"
    "v_pk_mul_f32 v[112:113], v[112:113], v[56:57] op_sel_hi:[1,0]\n\t"
    "v_pk_mul_f32 v[114:115], v[114:115], v[56:57] op_sel_hi:[1,0]\n\t"
    "v_pk_mul_f32 v[116:117], v[116:117], v[56:57] op_sel_hi:[1,0]\n\t"
    "v_pk_mul_f32 v[118:119], v[118:119], v[56:57] op_sel_hi:[1,0]\n\t"
    "v_cvt_pk_bf16_f32 v112, v112, v113\n\t"
    "v_cvt_pk_bf16_f32 v113, v114, v115\n\t"
    "v_cvt_pk_bf16_f32 v114, v116, v117\n\t"
    "v_cvt_pk_bf16_f32 v115, v118, v119\n\t"
    "v_pk_mul_f32 v[120:121], v[120:121], v[56:57] op_sel_hi:[1,0]\n\t"
    "v_pk_mul_f32 v[122:123], v[122:123], v[56:57] op_sel_hi:[1,0]\n\t"
    "v_pk_mul_f32 v[124:125], v[124:125], v[56:57] op_sel_hi:[1,0]\n\t"
    "v_pk_mul_f32 v[126:127], v[126:127], v[56:57] op_sel_hi:[1,0]\n\t"
    "v_cvt_pk_bf16_f32 v120, v120, v121\n\t"
    "v_cvt_pk_bf16_f32 v121, v122, v123\n\t"
    "v_cvt_pk_bf16_f32 v122, v124, v125\n\t"
    "v_cvt_pk_bf16_f32 v123, v126, v127\n\t"
    "v_mfma_f32_16x16x32_bf16 v[128:131], a[160:163], v[8:11], v[128:131]\n\t"
    "v_exp_f32_e32 v19, v19\n\t"
    "v_mfma_f32_16x16x32_bf16 v[132:135], a[164:167], v[8:11], v[132:135]\n\t"
    "ds_read_b64_tr_b16 a[144:145], v82 offset:8192\n\t"
    "ds_write_b128 v224, v[96:99] offset:0\n\t"
    "v_mfma_f32_16x16x32_bf16 v[136:139], a[168:171], v[8:11], v[136:139]\n\t"
    "v_exp_f32_e32 v18, v18\n\t"
    "v_mfma_f32_16x16x32_bf16 v[140:143], a[172:175], v[8:11], v[140:143]\n\t"
    "ds_read_b64_tr_b16 a[146:147], v82 offset:12288\n\t"
    "v_cvt_pk_bf16_f32 v21, v18, v19\n\t"
    "v_mfma_f32_16x16x32_bf16 v[144:147], a[176:179], v[8:11], v[144:147]\n\t"
    "v_exp_f32_e32 v17, v17\n\t"
    "v_mfma_f32_16x16x32_bf16 v[148:151], a[180:183], v[8:11], v[148:151]\n\t"
    "ds_read_b64_tr_b16 a[148:149], v82 offset:8200\n\t"
    "v_mfma_f32_16x16x32_bf16 v[152:155], a[184:187], v[8:11], v[152:155]\n\t"
    "v_exp_f32_e32 v16, v16\n\t"
    "v_mfma_f32_16x16x32_bf16 v[156:159], a[188:191], v[8:11], v[156:159]\n\t"
    "ds_read_b64_tr_b16 a[150:151], v82 offset:12296\n\t"
    "v_cvt_pk_bf16_f32 v20, v16, v17\n\t"
    "v_mfma_f32_16x16x32_bf16 v[56:59], v[236:239], v[8:11], v[56:59]\n\t"
    "ds_write_b128 v225, v[104:107] offset:0\n\t"
    "v_mfma_f32_16x16x32_bf16 v[0:3], a[96:99], a[64:67], v[48:51]\n\t"
    "v_exp_f32_e32 v24, v24\n\t"
    "v_mfma_f32_16x16x32_bf16 v[0:3], a[100:103], a[68:71], v[0:3]\n\t"
    "ds_read_b64_tr_b16 a[152:153], v83 offset:8192\n\t"
    "v_mfma_f32_16x16x32_bf16 v[0:3], a[104:107], a[72:75], v[0:3]\n\t"
    "v_exp_f32_e32 v25, v25\n\t"
    "v_mfma_f32_16x16x32_bf16 v[0:3], a[108:111], a[76:79], v[0:3]\n\t"
    "ds_read_b64_tr_b16 a[154:155], v83 offset:12288\n\t"
    "v_cvt_pk_bf16_f32 v24, v24, v25\n\t"
    "v_mfma_f32_16x16x32_bf16 v[4:7], a[112:115], a[64:67], v[48:51]\n\t"
    "v_exp_f32_e32 v26, v26\n\t"
    "v_mfma_f32_16x16x32_bf16 v[4:7], a[116:119], a[68:71], v[4:7]\n\t"
    "ds_read_b64_tr_b16 a[156:157], v83 offset:8200\n\t"
    "ds_write_b128 v226, v[112:115] offset:0\n\t"
    "v_mfma_f32_16x16x32_bf16 v[4:7], a[120:123], a[72:75], v[4:7]\n\t"
    "v_exp_f32_e32 v27, v27\n\t"
    "v_mfma_f32_16x16x32_bf16 v[4:7], a[124:127], a[76:79], v[4:7]\n\t"
    "ds_read_b64_tr_b16 a[158:159], v83 offset:12296\n\t"
    "v_cvt_pk_bf16_f32 v25, v26, v27\n\t"
    "v_add_f32_e32 v58, v59, v58\n\t"
    "v_cmp_ngt_f32_e32 vcc, s70, v58\n\t"
    "v_rcp_f32_e32 v58, v58\n\t"
    "s_or_b64 s[72:73], s[72:73], vcc\n\t"
    "v_pk_mul_f32 v[128:129], v[128:129], v[58:59] op_sel_hi:[1,0]\n\t"
    "v_pk_mul_f32 v[130:131], v[130:131], v[58:59] op_sel_hi:[1,0]\n\t"
    "v_pk_mul_f32 v[132:133], v[132:133], v[58:59] op_sel_hi:[1,0]\n\t"
    "v_pk_mul_f32 v[134:135], v[134:135], v[58:59] op_sel_hi:[1,0]\n\t"
    "v_cvt_pk_bf16_f32 v128, v128, v129\n\t"
    "v_cvt_pk_bf16_f32 v129, v130, v131\n\t"
    "v_cvt_pk_bf16_f32 v130, v132, v133\n\t"
    "v_cvt_pk_bf16_f32 v131, v134, v135\n\t"
    "v_pk_mul_f32 v[136:137], v[136:137], v[58:59] op_sel_hi:[1,0]\n\t"
    "v_pk_mul_f32 v[138:139], v[138:139], v[58:59] op_sel_hi:[1,0]\n\t"
    "v_pk_mul_f32 v[140:141], v[140:141], v[58:59] op_sel_hi:[1,0]\n\t"
    "v_pk_mul_f32 v[142:143], v[142:143], v[58:59] op_sel_hi:[1,0]\n\t"
    "v_cvt_pk_bf16_f32 v136, v136, v137\n\t"
    "v_cvt_pk_bf16_f32 v137, v138, v139\n\t"
    "v_cvt_pk_bf16_f32 v138, v140, v141\n\t"
    "v_cvt_pk_bf16_f32 v139, v142, v143\n\t"
    "v_pk_mul_f32 v[144:145], v[144:145], v[58:59] op_sel_hi:[1,0]\n\t"
    "v_pk_mul_f32 v[146:147], v[146:147], v[58:59] op_sel_hi:[1,0]\n\t"
    "v_pk_mul_f32 v[148:149], v[148:149], v[58:59] op_sel_hi:[1,0]\n\t"
    "v_pk_mul_f32 v[150:151], v[150:151], v[58:59] op_sel_hi:[1,0]\n\t"
    "v_cvt_pk_bf16_f32 v144, v144, v145\n\t"
    "v_cvt_pk_bf16_f32 v145, v146, v147\n\t"
    "v_cvt_pk_bf16_f32 v146, v148, v149\n\t"
    "v_cvt_pk_bf16_f32 v147, v150, v151\n\t"
    "v_pk_mul_f32 v[152:153], v[152:153], v[58:59] op_sel_hi:[1,0]\n\t"
    "v_pk_mul_f32 v[154:155], v[154:155], v[58:59] op_sel_hi:[1,0]\n\t"
    "v_pk_mul_f32 v[156:157], v[156:157], v[58:59] op_sel_hi:[1,0]\n\t"
    "v_pk_mul_f32 v[158:159], v[158:159], v[58:59] op_sel_hi:[1,0]\n\t"
    "v_cvt_pk_bf16_f32 v152, v152, v153\n\t"
    "v_cvt_pk_bf16_f32 v153, v154, v155\n\t"
    "v_cvt_pk_bf16_f32 v154, v156, v157\n\t"
    "v_cvt_pk_bf16_f32 v155, v158, v159\n\t"
    "v_mfma_f32_16x16x32_bf16 v[160:163], a[160:163], v[20:23], v[160:163]\n\t"
    "v_exp_f32_e32 v28, v28\n\t"
    "v_mfma_f32_16x16x32_bf16 v[164:167], a[164:167], v[20:23], v[164:167]\n\t"
    "ds_read_b64_tr_b16 a[160:161], v80 offset:32768\n\t"
    "ds_write_b128 v227, v[120:123] offset:0\n\t"
    "v_mfma_f32_16x16x32_bf16 v[168:171], a[168:171], v[20:23], v[168:171]\n\t"
    "v_exp_f32_e32 v29, v29\n\t"
    "v_mfma_f32_16x16x32_bf16 v[172:175], a[172:175], v[20:23], v[172:175]\n\t"
    "ds_read_b64_tr_b16 a[162:163], v80 offset:36864\n\t"
    "v_cvt_pk_bf16_f32 v26, v28, v29\n\t"
    "v_mfma_f32_16x16x32_bf16 v[176:179], a[176:179], v[20:23], v[176:179]\n\t"
    "v_exp_f32_e32 v30, v30\n\t"
    "v_mfma_f32_16x16x32_bf16 v[180:183], a[180:183], v[20:23], v[180:183]\n\t"
    "ds_read_b64_tr_b16 a[164:165], v80 offset:32776\n\t"
    "ds_read_b128 v[96:99], v84 offset:0\n\t"
    "v_mfma_f32_16x16x32_bf16 v[184:187], a[184:187], v[20:23], v[184:187]\n\t"
    "ds_read_b128 v[104:107], v84 offset:1024\n\t"
    "v_exp_f32_e32 v31, v31\n\t"
    "v_mfma_f32_16x16x32_bf16 v[188:191], a[188:191], v[20:23], v[188:191]\n\t"
    "ds_read_b64_tr_b16 a[166:167], v80 offset:36872\n\t"
    "v_cvt_pk_bf16_f32 v27, v30, v31\n\t"
    "v_mfma_f32_16x16x32_bf16 v[60:63], v[232:235], v[20:23], v[60:63]\n\t"
    "ds_read_b128 v[112:115], v84 offset:2048\n\t"
    "v_mfma_f32_16x16x32_bf16 v[8:11], a[96:99], a[80:83], v[52:55]\n\t"
    "ds_read_b128 v[120:123], v84 offset:3072\n\t"
    "v_exp_f32_e32 v7, v7\n\t"
    "v_mfma_f32_16x16x32_bf16 v[8:11], a[100:103], a[84:87], v[8:11]\n\t"
    "ds_read_b128 a[96:99], v76 offset:32768\n\t"
    "ds_write_b128 v224, v[128:131] offset:16384\n\t"
    "s_waitcnt lgkmcnt(11)\n\t"
    "v_mfma_f32_16x16x32_bf16 v[8:11], a[104:107], a[88:91], v[8:11]\n\t"
    "v_exp_f32_e32 v6, v6\n\t"
    "v_mfma_f32_16x16x32_bf16 v[8:11], a[108:111], a[92:95], v[8:11]\n\t"
    "ds_read_b128 a[100:103], v77 offset:32768\n\t"
    "v_cvt_pk_bf16_f32 v7, v6, v7\n\t"
    "v_mfma_f32_16x16x32_bf16 v[12:15], a[112:115], a[80:83], v[52:55]\n\t"
    "v_exp_f32_e32 v5, v5\n\t"
    "v_mfma_f32_16x16x32_bf16 v[12:15], a[116:119], a[84:87], v[12:15]\n\t"
    "ds_read_b128 a[104:107], v78 offset:32768\n\t"
    "ds_read_b128 a[108:111], v79 offset:32768\n\t"
    "ds_write_b128 v225, v[136:139] offset:16384\n\t"
    "v_mfma_f32_16x16x32_bf16 v[12:15], a[120:123], a[88:91], v[12:15]\n\t"
    "v_exp_f32_e32 v4, v4\n\t"
    "s_waitcnt lgkmcnt(6)\n\t"
    "v_mfma_f32_16x16x32_bf16 v[12:15], a[124:127], a[92:95], v[12:15]\n\t"
    "ds_read_b128 a[112:115], v76 offset:36864\n\t"
    "v_cvt_pk_bf16_f32 v6, v4, v5\n\t"
    "s_add_u32 s67, s71, 0\n\t"
    "v_add_f32_e32 v60, v61, v60\n\t"
    "v_cmp_ngt_f32_e32 vcc, s70, v60\n\t"
    "v_rcp_f32_e32 v60, v60\n\t"
    "s_or_b64 s[72:73], s[72:73], vcc\n\t"
    "v_pk_mul_f32 v[160:161], v[160:161], v[60:61] op_sel_hi:[1,0]\n\t"
    "v_pk_mul_f32 v[162:163], v[162:163], v[60:61] op_sel_hi:[1,0]\n\t"
    "v_pk_mul_f32 v[164:165], v[164:165], v[60:61] op_sel_hi:[1,0]\n\t"
    "v_pk_mul_f32 v[166:167], v[166:167], v[60:61] op_sel_hi:[1,0]\n\t"
    "v_cvt_pk_bf16_f32 v160, v160, v161\n\t"
    "v_cvt_pk_bf16_f32 v161, v162, v163\n\t"
    "v_cvt_pk_bf16_f32 v162, v164, v165\n\t"
    "v_cvt_pk_bf16_f32 v163, v166, v167\n\t"
    "v_pk_mul_f32 v[168:169], v[168:169], v[60:61] op_sel_hi:[1,0]\n\t"
    "v_pk_mul_f32 v[170:171], v[170:171], v[60:61] op_sel_hi:[1,0]\n\t"
    "v_pk_mul_f32 v[172:173], v[172:173], v[60:61] op_sel_hi:[1,0]\n\t"
    "v_pk_mul_f32 v[174:175], v[174:175], v[60:61] op_sel_hi:[1,0]\n\t"
    "v_cvt_pk_bf16_f32 v168, v168, v169\n\t"
    "v_cvt_pk_bf16_f32 v169, v170, v171\n\t"
    "v_cvt_pk_bf16_f32 v170, v172, v173\n\t"
    "v_cvt_pk_bf16_f32 v171, v174, v175\n\t"
    "v_pk_mul_f32 v[176:177], v[176:177], v[60:61] op_sel_hi:[1,0]\n\t"
    "v_pk_mul_f32 v[178:179], v[178:179], v[60:61] op_sel_hi:[1,0]\n\t"
    "v_pk_mul_f32 v[180:181], v[180:181], v[60:61] op_sel_hi:[1,0]\n\t"
    "v_pk_mul_f32 v[182:183], v[182:183], v[60:61] op_sel_hi:[1,0]\n\t"
    "v_cvt_pk_bf16_f32 v176, v176, v177\n\t"
    "v_cvt_pk_bf16_f32 v177, v178, v179\n\t"
    "v_cvt_pk_bf16_f32 v178, v180, v181\n\t"
    "v_cvt_pk_bf16_f32 v179, v182, v183\n\t"
    "v_pk_mul_f32 v[184:185], v[184:185], v[60:61] op_sel_hi:[1,0]\n\t"
    "v_pk_mul_f32 v[186:187], v[186:187], v[60:61] op_sel_hi:[1,0]\n\t"
    "v_pk_mul_f32 v[188:189], v[188:189], v[60:61] op_sel_hi:[1,0]\n\t"
    "v_pk_mul_f32 v[190:191], v[190:191], v[60:61] op_sel_hi:[1,0]\n\t"
    "v_cvt_pk_bf16_f32 v184, v184, v185\n\t"
    "v_cvt_pk_bf16_f32 v185, v186, v187\n\t"
    "v_cvt_pk_bf16_f32 v186, v188, v189\n\t"
    "v_cvt_pk_bf16_f32 v187, v190, v191\n\t"
    "v_mfma_f32_16x16x32_bf16 a[192:195], a[128:131], v[24:27], a[192:195]\n\t"
    "buffer_store_dwordx4 v[96:99], v88, s[52:55], s67 offen offset:0 sc1\n\t"
    "v_exp_f32_e32 v3, v3\n\t"
    "v_mfma_f32_16x16x32_bf16 a[196:199], a[132:135], v[24:27], a[196:199]\n\t"
    "ds_read_b128 a[116:119], v77 offset:36864\n\t"
    "ds_read_b128 a[120:123], v78 offset:36864\n\t"
    "ds_write_b128 v226, v[144:147] offset:16384\n\t"
    "v_mfma_f32_16x16x32_bf16 a[200:203], a[136:139], v[24:27], a[200:203]\n\t"
    "v_exp_f32_e32 v2, v2\n\t"
    "v_mfma_f32_16x16x32_bf16 a[204:207], a[140:143], v[24:27], a[204:207]\n\t"
    "ds_read_b128 a[124:127], v79 offset:36864\n\t"
    "v_cvt_pk_bf16_f32 v5, v2, v3\n\t"
    "v_mfma_f32_16x16x32_bf16 a[208:211], a[144:147], v[24:27], a[208:211]\n\t"
    "v_exp_f32_e32 v1, v1\n\t"
    "v_mfma_f32_16x16x32_bf16 a[212:215], a[148:151], v[24:27], a[212:215]\n\t"
    "ds_write_b128 v227, v[152:155] offset:16384\n\t"
    "v_mfma_f32_16x16x32_bf16 a[216:219], a[152:155], v[24:27], a[216:219]\n\t"
    "v_exp_f32_e32 v0, v0\n\t"
    "v_mfma_f32_16x16x32_bf16 a[220:223], a[156:159], v[24:27], a[220:223]\n\t"
    "v_cvt_pk_bf16_f32 v4, v0, v1\n\t"
    "v_mfma_f32_16x16x32_bf16 v[60:63], v[236:239], v[24:27], v[60:63]\n\t"
    "buffer_store_dwordx4 v[104:107], v89, s[52:55], s67 offen offset:1024 sc1\n\t"
    "s_waitcnt lgkmcnt(4)\n\t"
    "v_mfma_f32_16x16x32_bf16 v[16:19], a[96:99], a[48:51], v[44:47]\n\t"
    "v_exp_f32_e32 v8, v8\n\t"
    "v_mfma_f32_16x16x32_bf16 v[16:19], a[100:103], a[52:55], v[16:19]\n\t"
    "ds_read_b64_tr_b16 a[168:169], v81 offset:32768\n\t"
    "ds_read_b128 v[128:131], v84 offset:16384\n\t"
    "v_mfma_f32_16x16x32_bf16 v[16:19], a[104:107], a[56:59], v[16:19]\n\t"
    "ds_read_b128 v[136:139], v84 offset:17408\n\t"
    "v_exp_f32_e32 v9, v9\n\t"
    "v_mfma_f32_16x16x32_bf16 v[16:19], a[108:111], a[60:63], v[16:19]\n\t"
    "ds_read_b64_tr_b16 a[170:171], v81 offset:36864\n\t"
    "ds_read_b128 v[144:147], v84 offset:18432\n\t"
    "v_cvt_pk_bf16_f32 v8, v8, v9\n\t"
    "v_mfma_f32_16x16x32_bf16 v[20:23], a[112:115], a[48:51], v[44:47]\n\t"
    "ds_read_b128 v[152:155], v84 offset:19456\n\t"
    "v_exp_f32_e32 v10, v10\n\t"
    "v_mfma_f32_16x16x32_bf16 v[20:23], a[116:119], a[52:55], v[20:23]\n\t"
    "ds_read_b64_tr_b16 a[172:173], v81 offset:32776\n\t"
    "ds_write_b128 v224, v[160:163] offset:32768\n\t"
    "s_waitcnt lgkmcnt(9)\n\t"
    "v_mfma_f32_16x16x32_bf16 v[20:23], a[120:123], a[56:59], v[20:23]\n\t"
    "v_exp_f32_e32 v11, v11\n\t"
    "v_mfma_f32_16x16x32_bf16 v[20:23], a[124:127], a[60:63], v[20:23]\n\t"
    "ds_read_b64_tr_b16 a[174:175], v81 offset:36872\n\t"
    "buffer_store_dwordx4 v[112:115], v90, s[52:55], s67 offen offset:2048 sc1\n\t"
    "v_cvt_pk_bf16_f32 v9, v10, v11\n\t"
    "v_mfma_f32_16x16x32_bf16 a[224:227], a[128:131], v[4:7], a[224:227]\n\t"
    "v_exp_f32_e32 v12, v12\n\t"
    "v_mfma_f32_16x16x32_bf16 a[228:231], a[132:135], v[4:7], a[228:231]\n\t"
    "ds_read_b64_tr_b16 a[176:177], v82 offset:32768\n\t"
    "ds_write_b128 v225, v[168:171] offset:32768\n\t"
    "v_mfma_f32_16x16x32_bf16 a[232:235], a[136:139], v[4:7], a[232:235]\n\t"
    "v_exp_f32_e32 v13, v13\n\t"
    "v_mfma_f32_16x16x32_bf16 a[236:239], a[140:143], v[4:7], a[236:239]\n\t"
    "ds_read_b64_tr_b16 a[178:179], v82 offset:36864\n\t"
    "v_cvt_pk_bf16_f32 v10, v12, v13\n\t"
    "v_mfma_f32_16x16x32_bf16 a[240:243], a[144:147], v[4:7], a[240:243]\n\t"
    "v_exp_f32_e32 v14, v14\n\t"
    "v_mfma_f32_16x16x32_bf16 a[244:247], a[148:151], v[4:7], a[244:247]\n\t"
    "ds_read_b64_tr_b16 a[180:181], v82 offset:32776\n\t"
    "v_mfma_f32_16x16x32_bf16 a[248:251], a[152:155], v[4:7], a[248:251]\n\t"
    "v_exp_f32_e32 v15, v15\n\t"
    "v_mfma_f32_16x16x32_bf16 a[252:255], a[156:159], v[4:7], a[252:255]\n\t"
    "ds_read_b64_tr_b16 a[182:183], v82 offset:36872\n\t"
    "v_cvt_pk_bf16_f32 v11, v14, v15\n\t"
    "v_mfma_f32_16x16x32_bf16 v[64:67], v[232:235], v[4:7], v[64:67]\n\t"
    "ds_write_b128 v226, v[176:179] offset:32768\n\t"
    "v_mfma_f32_16x16x32_bf16 v[24:27], a[96:99], a[64:67], v[48:51]\n\t"
    "v_exp_f32_e32 v23, v23\n\t"
    "v_mfma_f32_16x16x32_bf16 v[24:27], a[100:103], a[68:71], v[24:27]\n\t"
    "ds_read_b64_tr_b16 a[184:185], v83 offset:32768\n\t"
    "buffer_store_dwordx4 v[120:123], v91, s[52:55], s67 offen offset:3072 sc1\n\t"
    "v_mfma_f32_16x16x32_bf16 v[24:27], a[104:107], a[72:75], v[24:27]\n\t"
    "v_exp_f32_e32 v22, v22\n\t"
    "v_mfma_f32_16x16x32_bf16 v[24:27], a[108:111], a[76:79], v[24:27]\n\t"
    "ds_read_b64_tr_b16 a[186:187], v83 offset:36864\n\t"
    "v_cvt_pk_bf16_f32 v23, v22, v23\n\t"
    "v_mfma_f32_16x16x32_bf16 v[28:31], a[112:115], a[64:67], v[48:51]\n\t"
    "v_exp_f32_e32 v21, v21\n\t"
    "v_mfma_f32_16x16x32_bf16 v[28:31], a[116:119], a[68:71], v[28:31]\n\t"
    "ds_read_b64_tr_b16 a[188:189], v83 offset:32776\n\t"
    "ds_write_b128 v227, v[184:187] offset:32768\n\t"
    "v_mfma_f32_16x16x32_bf16 v[28:31], a[120:123], a[72:75], v[28:31]\n\t"
    "v_exp_f32_e32 v20, v20\n\t"
    "s_waitcnt lgkmcnt(13)\n\t"
    "v_mfma_f32_16x16x32_bf16 v[28:31], a[124:127], a[76:79], v[28:31]\n\t"
    "ds_read_b64_tr_b16 a[190:191], v83 offset:36872\n\t"
    "v_cvt_pk_bf16_f32 v22, v20, v21\n\t"
    "s_add_u32 s67, s71, 0x1000\n\t"
    "v_mfma_f32_16x16x32_bf16 v[192:195], a[128:131], v[8:11], v[192:195]\n\t"
    "buffer_store_dwordx4 v[128:131], v88, s[52:55], s67 offen offset:0 sc1\n\t"
    "v_exp_f32_e32 v19, v19\n\t"
    "v_mfma_f32_16x16x32_bf16 v[196:199], a[132:135], v[8:11], v[196:199]\n\t"
    "ds_read_b64_tr_b16 a[128:129], v80 offset:40960\n\t"
    "ds_read_b128 v[160:163], v84 offset:32768\n\t"
    "v_mfma_f32_16x16x32_bf16 v[200:203], a[136:139], v[8:11], v[200:203]\n\t"
    "ds_read_b128 v[168:171], v84 offset:33792\n\t"
    "v_exp_f32_e32 v18, v18\n\t"
    "v_mfma_f32_16x16x32_bf16 v[204:207], a[140:143], v[8:11], v[204:207]\n\t"
    "ds_read_b64_tr_b16 a[130:131], v80 offset:45056\n\t"
    "ds_read_b128 v[176:179], v84 offset:34816\n\t"
    "v_cvt_pk_bf16_f32 v21, v18, v19\n\t"
    "v_mfma_f32_16x16x32_bf16 v[208:211], a[144:147], v[8:11], v[208:211]\n\t"
    "v_exp_f32_e32 v17, v17\n\t"
    "v_mfma_f32_16x16x32_bf16 v[212:215], a[148:151], v[8:11], v[212:215]\n\t"
    "ds_read_b64_tr_b16 a[132:133], v80 offset:40968\n\t"
    "ds_read_b128 v[184:187], v84 offset:35840\n\t"
    "v_mfma_f32_16x16x32_bf16 v[216:219], a[152:155], v[8:11], v[216:219]\n\t"
    "v_exp_f32_e32 v16, v16\n\t"
    "v_mfma_f32_16x16x32_bf16 v[220:223], a[156:159], v[8:11], v[220:223]\n\t"
    "ds_read_b64_tr_b16 a[134:135], v80 offset:45064\n\t"
    "v_cvt_pk_bf16_f32 v20, v16, v17\n\t"
    "v_mfma_f32_16x16x32_bf16 v[64:67], v[236:239], v[8:11], v[64:67]\n\t"
    "buffer_store_dwordx4 v[136:139], v89, s[52:55], s67 offen offset:1024 sc1\n\t"
    "v_mfma_f32_16x16x32_bf16 v[0:3], a[96:99], a[80:83], v[52:55]\n\t"
    "v_exp_f32_e32 v24, v24\n\t"
    "v_mfma_f32_16x16x32_bf16 v[0:3], a[100:103], a[84:87], v[0:3]\n\t"
    "ds_read_b128 a[96:99], v76 offset:40960\n\t"
    "s_waitcnt lgkmcnt(9)\n\t"
    "v_mfma_f32_16x16x32_bf16 v[0:3], a[104:107], a[88:91], v[0:3]\n\t"
    "v_exp_f32_e32 v25, v25\n\t"
    "v_mfma_f32_16x16x32_bf16 v[0:3], a[108:111], a[92:95], v[0:3]\n\t"
    "ds_read_b128 a[100:103], v77 offset:40960\n\t"
    "v_cvt_pk_bf16_f32 v24, v24, v25\n\t"
    "v_mfma_f32_16x16x32_bf16 v[4:7], a[112:115], a[80:83], v[52:55]\n\t"
    "v_exp_f32_e32 v26, v26\n\t"
    "v_mfma_f32_16x16x32_bf16 v[4:7], a[116:119], a[84:87], v[4:7]\n\t"
    "ds_read_b128 a[104:107], v78 offset:40960\n\t"
    "ds_read_b128 a[108:111], v79 offset:40960\n\t"
    "v_mfma_f32_16x16x32_bf16 v[4:7], a[120:123], a[88:91], v[4:7]\n\t"
    "v_exp_f32_e32 v27, v27\n\t"
    "v_mfma_f32_16x16x32_bf16 v[4:7], a[124:127], a[92:95], v[4:7]\n\t"
    "ds_read_b128 a[112:115], v76 offset:45056\n\t"
    "buffer_store_dwordx4 v[144:147], v90, s[52:55], s67 offen offset:2048 sc1\n\t"
    "v_cvt_pk_bf16_f32 v25, v26, v27\n\t"
    "v_mfma_f32_16x16x32_bf16 a[192:195], a[160:163], v[20:23], a[192:195]\n\t"
    "v_exp_f32_e32 v28, v28\n\t"
    "v_mfma_f32_16x16x32_bf16 a[196:199], a[164:167], v[20:23], a[196:199]\n\t"
    "ds_read_b128 a[116:119], v77 offset:45056\n\t"
    "ds_read_b128 a[120:123], v78 offset:45056\n\t"
    "v_mfma_f32_16x16x32_bf16 a[200:203], a[168:171], v[20:23], a[200:203]\n\t"
    "v_exp_f32_e32 v29, v29\n\t"
    "v_mfma_f32_16x16x32_bf16 a[204:207], a[172:175], v[20:23], a[204:207]\n\t"
    "ds_read_b128 a[124:127], v79 offset:45056\n\t"
    "v_cvt_pk_bf16_f32 v26, v28, v29\n\t"
    "v_mfma_f32_16x16x32_bf16 a[208:211], a[176:179], v[20:23], a[208:211]\n\t"
    "v_exp_f32_e32 v30, v30\n\t"
    "v_mfma_f32_16x16x32_bf16 a[212:215], a[180:183], v[20:23], a[212:215]\n\t"
    "v_mfma_f32_16x16x32_bf16 a[216:219], a[184:187], v[20:23], a[216:219]\n\t"
    "v_exp_f32_e32 v31, v31\n\t"
    "v_mfma_f32_16x16x32_bf16 a[220:223], a[188:191], v[20:23], a[220:223]\n\t"
    "buffer_store_dwordx4 v[152:155], v91, s[52:55], s67 offen offset:3072 sc1\n\t"
    "v_cvt_pk_bf16_f32 v27, v30, v31\n\t"
    "v_mfma_f32_16x16x32_bf16 v[60:63], v[236:239], v[20:23], v[60:63]\n\t"
    "s_waitcnt lgkmcnt(2)\n\t"
    "v_mfma_f32_16x16x32_bf16 v[8:11], a[96:99], a[48:51], v[44:47]\n\t"
    "v_exp_f32_e32 v7, v7\n\t"
    "v_mfma_f32_16x16x32_bf16 v[8:11], a[100:103], a[52:55], v[8:11]\n\t"
    "ds_read_b64_tr_b16 a[136:137], v81 offset:40960\n\t"
    "v_mfma_f32_16x16x32_bf16 v[8:11], a[104:107], a[56:59], v[8:11]\n\t"
    "v_exp_f32_e32 v6, v6\n\t"
    "v_mfma_f32_16x16x32_bf16 v[8:11], a[108:111], a[60:63], v[8:11]\n\t"
    "ds_read_b64_tr_b16 a[138:139], v81 offset:45056\n\t"
    "v_cvt_pk_bf16_f32 v7, v6, v7\n\t"
    "s_waitcnt lgkmcnt(11)\n\t"
    "v_mfma_f32_16x16x32_bf16 v[12:15], a[112:115], a[48:51], v[44:47]\n\t"
    "v_exp_f32_e32 v5, v5\n\t"
    "s_add_u32 s67, s71, 0x2000\n\t"
    "v_mfma_f32_16x16x32_bf16 v[12:15], a[116:119], a[52:55], v[12:15]\n\t"
    "ds_read_b64_tr_b16 a[140:141], v81 offset:40968\n\t"
    "buffer_store_dwordx4 v[160:163], v88, s[52:55], s67 offen offset:0 sc1\n\t"
    "s_waitcnt lgkmcnt(3)\n\t"
    "v_mfma_f32_16x16x32_bf16 v[12:15], a[120:123], a[56:59], v[12:15]\n\t"
    "v_exp_f32_e32 v4, v4\n\t"
    "v_mfma_f32_16x16x32_bf16 v[12:15], a[124:127], a[60:63], v[12:15]\n\t"
    "ds_read_b64_tr_b16 a[142:143], v81 offset:45064\n\t"
    "v_cvt_pk_bf16_f32 v6, v4, v5\n\t"
    "v_mfma_f32_16x16x32_bf16 a[224:227], a[160:163], v[24:27], a[224:227]\n\t"
    "v_exp_f32_e32 v3, v3\n\t"
    "v_mfma_f32_16x16x32_bf16 a[228:231], a[164:167], v[24:27], a[228:231]\n\t"
    "ds_read_b64_tr_b16 a[144:145], v82 offset:40960\n\t"
    "v_mfma_f32_16x16x32_bf16 a[232:235], a[168:171], v[24:27], a[232:235]\n\t"
    "v_exp_f32_e32 v2, v2\n\t"
    "v_mfma_f32_16x16x32_bf16 a[236:239], a[172:175], v[24:27], a[236:239]\n\t"
    "ds_read_b64_tr_b16 a[146:147], v82 offset:45056\n\t"
    "v_cvt_pk_bf16_f32 v5, v2, v3\n\t"
    "v_mfma_f32_16x16x32_bf16 a[240:243], a[176:179], v[24:27], a[240:243]\n\t"
    "v_exp_f32_e32 v1, v1\n\t"
    "v_mfma_f32_16x16x32_bf16 a[244:247], a[180:183], v[24:27], a[244:247]\n\t"
    "ds_read_b64_tr_b16 a[148:149], v82 offset:40968\n\t"
    "v_mfma_f32_16x16x32_bf16 a[248:251], a[184:187], v[24:27], a[248:251]\n\t"
    "v_exp_f32_e32 v0, v0\n\t"
    "v_mfma_f32_16x16x32_bf16 a[252:255], a[188:191], v[24:27], a[252:255]\n\t"
    "ds_read_b64_tr_b16 a[150:151], v82 offset:45064\n\t"
    "v_cvt_pk_bf16_f32 v4, v0, v1\n\t"
    "v_mfma_f32_16x16x32_bf16 v[64:67], v[232:235], v[24:27], v[64:67]\n\t"
    "buffer_store_dwordx4 v[168:171], v89, s[52:55], s67 offen offset:1024 sc1\n\t"
    "v_mfma_f32_16x16x32_bf16 v[16:19], a[96:99], a[64:67], v[48:51]\n\t"
    "v_exp_f32_e32 v8, v8\n\t"
    "v_mfma_f32_16x16x32_bf16 v[16:19], a[100:103], a[68:71], v[16:19]\n\t"
    "ds_read_b64_tr_b16 a[152:153], v83 offset:40960\n\t"
    "v_mfma_f32_16x16x32_bf16 v[16:19], a[104:107], a[72:75], v[16:19]\n\t"
    "v_exp_f32_e32 v9, v9\n\t"
    "v_mfma_f32_16x16x32_bf16 v[16:19], a[108:111], a[76:79], v[16:19]\n\t"
    "ds_read_b64_tr_b16 a[154:155], v83 offset:45056\n\t"
    "v_cvt_pk_bf16_f32 v8, v8, v9\n\t"
    "v_mfma_f32_16x16x32_bf16 v[20:23], a[112:115], a[64:67], v[48:51]\n\t"
    "v_exp_f32_e32 v10, v10\n\t"
    "v_mfma_f32_16x16x32_bf16 v[20:23], a[116:119], a[68:71], v[20:23]\n\t"
    "ds_read_b64_tr_b16 a[156:157], v83 offset:40968\n\t"
    "v_mfma_f32_16x16x32_bf16 v[20:23], a[120:123], a[72:75], v[20:23]\n\t"
    "v_exp_f32_e32 v11, v11\n\t"
    "v_mfma_f32_16x16x32_bf16 v[20:23], a[124:127], a[76:79], v[20:23]\n\t"
    "ds_read_b64_tr_b16 a[158:159], v83 offset:45064\n\t"
    "v_cvt_pk_bf16_f32 v9, v10, v11\n\t"
    "v_mfma_f32_16x16x32_bf16 v[192:195], a[160:163], v[4:7], v[192:195]\n\t"
    "v_exp_f32_e32 v12, v12\n\t"
    "v_mfma_f32_16x16x32_bf16 v[196:199], a[164:167], v[4:7], v[196:199]\n\t"
    "buffer_store_dwordx4 v[176:179], v90, s[52:55], s67 offen offset:2048 sc1\n\t"
    "v_mfma_f32_16x16x32_bf16 v[200:203], a[168:171], v[4:7], v[200:203]\n\t"
    "v_exp_f32_e32 v13, v13\n\t"
    "v_mfma_f32_16x16x32_bf16 v[204:207], a[172:175], v[4:7], v[204:207]\n\t"
    "v_cvt_pk_bf16_f32 v10, v12, v13\n\t"
    "v_mfma_f32_16x16x32_bf16 v[208:211], a[176:179], v[4:7], v[208:211]\n\t"
    "v_exp_f32_e32 v14, v14\n\t"
    "v_mfma_f32_16x16x32_bf16 v[212:215], a[180:183], v[4:7], v[212:215]\n\t"
    "v_mfma_f32_16x16x32_bf16 v[216:219], a[184:187], v[4:7], v[216:219]\n\t"
    "v_exp_f32_e32 v15, v15\n\t"
    "v_mfma_f32_16x16x32_bf16 v[220:223], a[188:191], v[4:7], v[220:223]\n\t"
    "v_cvt_pk_bf16_f32 v11, v14, v15\n\t"
    "v_mfma_f32_16x16x32_bf16 v[64:67], v[236:239], v[4:7], v[64:67]\n\t"
    "v_mfma_f32_16x16x32_bf16 v[24:27], a[96:99], a[80:83], v[52:55]\n\t"
    "v_exp_f32_e32 v23, v23\n\t"
    "v_mfma_f32_16x16x32_bf16 v[24:27], a[100:103], a[84:87], v[24:27]\n\t"
    "buffer_store_dwordx4 v[184:187], v91, s[52:55], s67 offen offset:3072 sc1\n\t"
    "v_mfma_f32_16x16x32_bf16 v[24:27], a[104:107], a[88:91], v[24:27]\n\t"
    "v_exp_f32_e32 v22, v22\n\t"
    "v_mfma_f32_16x16x32_bf16 v[24:27], a[108:111], a[92:95], v[24:27]\n\t"
    "v_cvt_pk_bf16_f32 v23, v22, v23\n\t"
    "v_mfma_f32_16x16x32_bf16 v[28:31], a[112:115], a[80:83], v[52:55]\n\t"
    "v_exp_f32_e32 v21, v21\n\t"
    "v_mfma_f32_16x16x32_bf16 v[28:31], a[116:119], a[84:87], v[28:31]\n\t"
    "v_mfma_f32_16x16x32_bf16 v[28:31], a[120:123], a[88:91], v[28:31]\n\t"
    "v_exp_f32_e32 v20, v20\n\t"
    "v_mfma_f32_16x16x32_bf16 v[28:31], a[124:127], a[92:95], v[28:31]\n\t"
    "v_cvt_pk_bf16_f32 v22, v20, v21\n\t"
    "s_waitcnt lgkmcnt(0)\n\t"
    "v_mfma_f32_16x16x32_bf16 a[192:195], a[128:131], v[8:11], a[192:195]\n\t"
    "v_exp_f32_e32 v19, v19\n\t"
    "v_mfma_f32_16x16x32_bf16 a[196:199], a[132:135], v[8:11], a[196:199]\n\t"
    "v_mfma_f32_16x16x32_bf16 a[200:203], a[136:139], v[8:11], a[200:203]\n\t"
    "v_exp_f32_e32 v18, v18\n\t"
    "v_mfma_f32_16x16x32_bf16 a[204:207], a[140:143], v[8:11], a[204:207]\n\t"
    "v_cvt_pk_bf16_f32 v21, v18, v19\n\t"
    "v_mfma_f32_16x16x32_bf16 a[208:211], a[144:147], v[8:11], a[208:211]\n\t"
    "v_exp_f32_e32 v17, v17\n\t"
    "v_mfma_f32_16x16x32_bf16 a[212:215], a[148:151], v[8:11], a[212:215]\n\t"
    "v_mfma_f32_16x16x32_bf16 a[216:219], a[152:155], v[8:11], a[216:219]\n\t"
    "v_exp_f32_e32 v16, v16\n\t"
    "v_mfma_f32_16x16x32_bf16 a[220:223], a[156:159], v[8:11], a[220:223]\n\t"
    "v_cvt_pk_bf16_f32 v20, v16, v17\n\t"
    "v_mfma_f32_16x16x32_bf16 v[60:63], v[236:239], v[8:11], v[60:63]\n\t"
    "s_nop 0\n\t"
    "v_mfma_f32_16x16x32_bf16 a[224:227], a[128:131], v[20:23], a[224:227]\n\t"
    "v_exp_f32_e32 v24, v24\n\t"
    "v_mfma_f32_16x16x32_bf16 a[228:231], a[132:135], v[20:23], a[228:231]\n\t"
    "v_exp_f32_e32 v25, v25\n\t"
    "v_mfma_f32_16x16x32_bf16 a[232:235], a[136:139], v[20:23], a[232:235]\n\t"
    "v_exp_f32_e32 v26, v26\n\t"
    "v_cvt_pk_bf16_f32 v24, v24, v25\n\t"
    "v_mfma_f32_16x16x32_bf16 a[236:239], a[140:143], v[20:23], a[236:239]\n\t"
    "v_exp_f32_e32 v27, v27\n\t"
    "v_mfma_f32_16x16x32_bf16 a[240:243], a[144:147], v[20:23], a[240:243]\n\t"
    "v_exp_f32_e32 v28, v28\n\t"
    "v_cvt_pk_bf16_f32 v25, v26, v27\n\t"
    "v_mfma_f32_16x16x32_bf16 a[244:247], a[148:151], v[20:23], a[244:247]\n\t"
    "v_exp_f32_e32 v29, v29\n\t"
    "v_mfma_f32_16x16x32_bf16 a[248:251], a[152:155], v[20:23], a[248:251]\n\t"
    "v_exp_f32_e32 v30, v30\n\t"
    "v_cvt_pk_bf16_f32 v26, v28, v29\n\t"
    "v_mfma_f32_16x16x32_bf16 a[252:255], a[156:159], v[20:23], a[252:255]\n\t"
    "v_exp_f32_e32 v31, v31\n\t"
    "v_mfma_f32_16x16x32_bf16 v[64:67], v[232:235], v[20:23], v[64:67]\n\t"
    "v_cvt_pk_bf16_f32 v27, v30, v31\n\t"
    "v_add_f32_e32 v62, v63, v62\n\t"
    "v_cmp_ngt_f32_e32 vcc, s70, v62\n\t"
    "v_mfma_f32_16x16x32_bf16 v[192:195], a[128:131], v[24:27], v[192:195]\n\t"
    "v_rcp_f32_e32 v62, v62\n\t"
    "s_or_b64 s[72:73], s[72:73], vcc\n\t"
    "v_mfma_f32_16x16x32_bf16 v[196:199], a[132:135], v[24:27], v[196:199]\n\t"
    "v_accvgpr_read_b32 v96, a192\n\t"
    "v_accvgpr_read_b32 v97, a193\n\t"
    "v_mfma_f32_16x16x32_bf16 v[200:203], a[136:139], v[24:27], v[200:203]\n\t"
    "v_accvgpr_read_b32 v98, a194\n\t"
    "v_accvgpr_read_b32 v99, a195\n\t"
    "v_mfma_f32_16x16x32_bf16 v[204:207], a[140:143], v[24:27], v[204:207]\n\t"
    "v_accvgpr_read_b32 v100, a196\n\t"
    "v_accvgpr_read_b32 v101, a197\n\t"
    "v_mfma_f32_16x16x32_bf16 v[208:211], a[144:147], v[24:27], v[208:211]\n\t"
    "v_accvgpr_read_b32 v102, a198\n\t"
    "v_accvgpr_read_b32 v103, a199\n\t"
    "v_mfma_f32_16x16x32_bf16 v[212:215], a[148:151], v[24:27], v[212:215]\n\t"
    "v_mul_f32_e32 v96, v62, v96\n\t"
    "v_mul_f32_e32 v97, v62, v97\n\t"
    "v_mfma_f32_16x16x32_bf16 v[216:219], a[152:155], v[24:27], v[216:219]\n\t"
    "v_mul_f32_e32 v98, v62, v98\n\t"
    "v_mul_f32_e32 v99, v62, v99\n\t"
    "v_mfma_f32_16x16x32_bf16 v[220:223], a[156:159], v[24:27], v[220:223]\n\t"
    "v_mul_f32_e32 v100, v62, v100\n\t"
    "v_mul_f32_e32 v101, v62, v101\n\t"
    "v_mfma_f32_16x16x32_bf16 v[64:67], v[236:239], v[24:27], v[64:67]\n\t"
    "v_mul_f32_e32 v102, v62, v102\n\t"
    "v_mul_f32_e32 v103, v62, v103\n\t"
    "v_cvt_pk_bf16_f32 v96, v96, v97\n\t"
    "v_cvt_pk_bf16_f32 v97, v98, v99\n\t"
    "v_cvt_pk_bf16_f32 v98, v100, v101\n\t"
    "v_cvt_pk_bf16_f32 v99, v102, v103\n\t"
    "ds_write_b128 v224, v[96:99] offset:49152\n\t"
    "v_accvgpr_read_b32 v104, a200\n\t"
    "v_accvgpr_read_b32 v105, a201\n\t"
    "v_accvgpr_read_b32 v106, a202\n\t"
    "v_accvgpr_read_b32 v107, a203\n\t"
    "v_accvgpr_read_b32 v108, a204\n\t"
    "v_accvgpr_read_b32 v109, a205\n\t"
    "v_accvgpr_read_b32 v110, a206\n\t"
    "v_accvgpr_read_b32 v111, a207\n\t"
    "v_pk_mul_f32 v[104:105], v[104:105], v[62:63] op_sel_hi:[1,0]\n\t"
    "v_pk_mul_f32 v[106:107], v[106:107], v[62:63] op_sel_hi:[1,0]\n\t"
    "v_pk_mul_f32 v[108:109], v[108:109], v[62:63] op_sel_hi:[1,0]\n\t"
    "v_pk_mul_f32 v[110:111], v[110:111], v[62:63] op_sel_hi:[1,0]\n\t"
    "v_cvt_pk_bf16_f32 v104, v104, v105\n\t"
    "v_cvt_pk_bf16_f32 v105, v106, v107\n\t"
    "v_cvt_pk_bf16_f32 v106, v108, v109\n\t"
    "v_cvt_pk_bf16_f32 v107, v110, v111\n\t"
    "ds_write_b128 v225, v[104:107] offset:49152\n\t"
    "v_accvgpr_read_b32 v112, a208\n\t"
    "v_accvgpr_read_b32 v113, a209\n\t"
    "v_accvgpr_read_b32 v114, a210\n\t"
    "v_accvgpr_read_b32 v115, a211\n\t"
    "v_accvgpr_read_b32 v116, a212\n\t"
    "v_accvgpr_read_b32 v117, a213\n\t"
    "v_accvgpr_read_b32 v118, a214\n\t"
    "v_accvgpr_read_b32 v119, a215\n\t"
    "v_pk_mul_f32 v[112:113], v[112:113], v[62:63] op_sel_hi:[1,0]\n\t"
    "v_pk_mul_f32 v[114:115], v[114:115], v[62:63] op_sel_hi:[1,0]\n\t"
    "v_pk_mul_f32 v[116:117], v[116:117], v[62:63] op_sel_hi:[1,0]\n\t"
    "v_pk_mul_f32 v[118:119], v[118:119], v[62:63] op_sel_hi:[1,0]\n\t"
    "v_cvt_pk_bf16_f32 v112, v112, v113\n\t"
    "v_cvt_pk_bf16_f32 v113, v114, v115\n\t"
    "v_cvt_pk_bf16_f32 v114, v116, v117\n\t"
    "v_cvt_pk_bf16_f32 v115, v118, v119\n\t"
    "ds_write_b128 v226, v[112:115] offset:49152\n\t"
    "v_accvgpr_read_b32 v120, a216\n\t"
    "v_accvgpr_read_b32 v121, a217\n\t"
    "v_accvgpr_read_b32 v122, a218\n\t"
    "v_accvgpr_read_b32 v123, a219\n\t"
    "v_accvgpr_read_b32 v124, a220\n\t"
    "v_accvgpr_read_b32 v125, a221\n\t"
    "v_accvgpr_read_b32 v126, a222\n\t"
    "v_accvgpr_read_b32 v127, a223\n\t"
    "v_pk_mul_f32 v[120:121], v[120:121], v[62:63] op_sel_hi:[1,0]\n\t"
    "v_pk_mul_f32 v[122:123], v[122:123], v[62:63] op_sel_hi:[1,0]\n\t"
    "v_pk_mul_f32 v[124:125], v[124:125], v[62:63] op_sel_hi:[1,0]\n\t"
    "v_pk_mul_f32 v[126:127], v[126:127], v[62:63] op_sel_hi:[1,0]\n\t"
    "v_cvt_pk_bf16_f32 v120, v120, v121\n\t"
    "v_cvt_pk_bf16_f32 v121, v122, v123\n\t"
    "v_cvt_pk_bf16_f32 v122, v124, v125\n\t"
    "v_cvt_pk_bf16_f32 v123, v126, v127\n\t"
    "ds_write_b128 v227, v[120:123] offset:49152\n\t"
    "ds_read_b128 v[96:99], v84 offset:49152\n\t"
    "ds_read_b128 v[104:107], v84 offset:50176\n\t"
    "ds_read_b128 v[112:115], v84 offset:51200\n\t"
    "ds_read_b128 v[120:123], v84 offset:52224\n\t"
    "v_add_f32_e32 v64, v65, v64\n\t"
    "v_cmp_ngt_f32_e32 vcc, s70, v64\n\t"
    "v_rcp_f32_e32 v64, v64\n\t"
    "s_or_b64 s[72:73], s[72:73], vcc\n\t"
    "v_accvgpr_read_b32 v128, a224\n\t"
    "v_accvgpr_read_b32 v129, a225\n\t"
    "v_accvgpr_read_b32 v130, a226\n\t"
    "v_accvgpr_read_b32 v131, a227\n\t"
    "v_accvgpr_read_b32 v132, a228\n\t"
    "v_accvgpr_read_b32 v133, a229\n\t"
    "v_accvgpr_read_b32 v134, a230\n\t"
    "v_accvgpr_read_b32 v135, a231\n\t"
    "v_pk_mul_f32 v[128:129], v[128:129], v[64:65] op_sel_hi:[1,0]\n\t"
    "v_pk_mul_f32 v[130:131], v[130:131], v[64:65] op_sel_hi:[1,0]\n\t"
    "v_pk_mul_f32 v[132:133], v[132:133], v[64:65] op_sel_hi:[1,0]\n\t"
    "v_pk_mul_f32 v[134:135], v[134:135], v[64:65] op_sel_hi:[1,0]\n\t"
    "v_cvt_pk_bf16_f32 v128, v128, v129\n\t"
    "v_cvt_pk_bf16_f32 v129, v130, v131\n\t"
    "v_cvt_pk_bf16_f32 v130, v132, v133\n\t"
    "v_cvt_pk_bf16_f32 v131, v134, v135\n\t"
    "ds_write_b128 v228, v[128:131] offset:0\n\t"
    "v_accvgpr_read_b32 v136, a232\n\t"
    "v_accvgpr_read_b32 v137, a233\n\t"
    "v_accvgpr_read_b32 v138, a234\n\t"
    "v_accvgpr_read_b32 v139, a235\n\t"
    "v_accvgpr_read_b32 v140, a236\n\t"
    "v_accvgpr_read_b32 v141, a237\n\t"
    "v_accvgpr_read_b32 v142, a238\n\t"
    "v_accvgpr_read_b32 v143, a239\n\t"
    "v_pk_mul_f32 v[136:137], v[136:137], v[64:65] op_sel_hi:[1,0]\n\t"
    "v_pk_mul_f32 v[138:139], v[138:139], v[64:65] op_sel_hi:[1,0]\n\t"
    "v_pk_mul_f32 v[140:141], v[140:141], v[64:65] op_sel_hi:[1,0]\n\t"
    "v_pk_mul_f32 v[142:143], v[142:143], v[64:65] op_sel_hi:[1,0]\n\t"
    "v_cvt_pk_bf16_f32 v136, v136, v137\n\t"
    "v_cvt_pk_bf16_f32 v137, v138, v139\n\t"
    "v_cvt_pk_bf16_f32 v138, v140, v141\n\t"
    "v_cvt_pk_bf16_f32 v139, v142, v143\n\t"
    "ds_write_b128 v229, v[136:139] offset:0\n\t"
    "s_waitcnt lgkmcnt(2)\n\t"
    "s_add_u32 s67, s71, 0x3000\n\t"
    "buffer_store_dwordx4 v[96:99], v88, s[52:55], s67 offen offset:0 sc1\n\t"
    "buffer_store_dwordx4 v[104:107], v89, s[52:55], s67 offen offset:1024 sc1\n\t"
    "buffer_store_dwordx4 v[112:115], v90, s[52:55], s67 offen offset:2048 sc1\n\t"
    "buffer_store_dwordx4 v[120:123], v91, s[52:55], s67 offen offset:3072 sc1\n\t"
    "v_accvgpr_read_b32 v144, a240\n\t"
    "v_accvgpr_read_b32 v145, a241\n\t"
    "v_accvgpr_read_b32 v146, a242\n\t"
    "v_accvgpr_read_b32 v147, a243\n\t"
    "v_accvgpr_read_b32 v148, a244\n\t"
    "v_accvgpr_read_b32 v149, a245\n\t"
    "v_accvgpr_read_b32 v150, a246\n\t"
    "v_accvgpr_read_b32 v151, a247\n\t"
    "v_pk_mul_f32 v[144:145], v[144:145], v[64:65] op_sel_hi:[1,0]\n\t"
    "v_pk_mul_f32 v[146:147], v[146:147], v[64:65] op_sel_hi:[1,0]\n\t"
    "v_pk_mul_f32 v[148:149], v[148:149], v[64:65] op_sel_hi:[1,0]\n\t"
    "v_pk_mul_f32 v[150:151], v[150:151], v[64:65] op_sel_hi:[1,0]\n\t"
    "v_cvt_pk_bf16_f32 v144, v144, v145\n\t"
    "v_cvt_pk_bf16_f32 v145, v146, v147\n\t"
    "v_cvt_pk_bf16_f32 v146, v148, v149\n\t"
    "v_cvt_pk_bf16_f32 v147, v150, v151\n\t"
    "ds_write_b128 v230, v[144:147] offset:0\n\t"
    "v_accvgpr_read_b32 v152, a248\n\t"
    "v_accvgpr_read_b32 v153, a249\n\t"
    "v_accvgpr_read_b32 v154, a250\n\t"
    "v_accvgpr_read_b32 v155, a251\n\t"
    "v_accvgpr_read_b32 v156, a252\n\t"
    "v_accvgpr_read_b32 v157, a253\n\t"
    "v_accvgpr_read_b32 v158, a254\n\t"
    "v_accvgpr_read_b32 v159, a255\n\t"
    "v_pk_mul_f32 v[152:153], v[152:153], v[64:65] op_sel_hi:[1,0]\n\t"
    "v_pk_mul_f32 v[154:155], v[154:155], v[64:65] op_sel_hi:[1,0]\n\t"
    "v_pk_mul_f32 v[156:157], v[156:157], v[64:65] op_sel_hi:[1,0]\n\t"
    "v_pk_mul_f32 v[158:159], v[158:159], v[64:65] op_sel_hi:[1,0]\n\t"
    "v_cvt_pk_bf16_f32 v152, v152, v153\n\t"
    "v_cvt_pk_bf16_f32 v153, v154, v155\n\t"
    "v_cvt_pk_bf16_f32 v154, v156, v157\n\t"
    "v_cvt_pk_bf16_f32 v155, v158, v159\n\t"
    "ds_write_b128 v231, v[152:155] offset:0\n\t"
    "ds_read_b128 v[128:131], v85 offset:0\n\t"
    "ds_read_b128 v[136:139], v85 offset:1024\n\t"
    "ds_read_b128 v[144:147], v85 offset:2048\n\t"
    "ds_read_b128 v[152:155], v85 offset:3072\n\t"
    "s_cmp_eq_u32 %[wid], 3\n\t"
    "s_cbranch_scc1 LS5_%=\n\t"
    "v_add_f32_e32 v66, v67, v66\n\t"
    "v_cmp_ngt_f32_e32 vcc, s70, v66\n\t"
    "v_rcp_f32_e32 v66, v66\n\t"
    "s_or_b64 s[72:73], s[72:73], vcc\n\t"
    "v_pk_mul_f32 v[192:193], v[192:193], v[66:67] op_sel_hi:[1,0]\n\t"
    "v_pk_mul_f32 v[194:195], v[194:195], v[66:67] op_sel_hi:[1,0]\n\t"
    "v_pk_mul_f32 v[196:197], v[196:197], v[66:67] op_sel_hi:[1,0]\n\t"
    "v_pk_mul_f32 v[198:199], v[198:199], v[66:67] op_sel_hi:[1,0]\n\t"
    "v_cvt_pk_bf16_f32 v192, v192, v193\n\t"
    "v_cvt_pk_bf16_f32 v193, v194, v195\n\t"
    "v_cvt_pk_bf16_f32 v194, v196, v197\n\t"
    "v_cvt_pk_bf16_f32 v195, v198, v199\n\t"
    "ds_write_b128 v228, v[192:195] offset:16384\n\t"
    "v_pk_mul_f32 v[200:201], v[200:201], v[66:67] op_sel_hi:[1,0]\n\t"
    "v_pk_mul_f32 v[202:203], v[202:203], v[66:67] op_sel_hi:[1,0]\n\t"
    "v_pk_mul_f32 v[204:205], v[204:205], v[66:67] op_sel_hi:[1,0]\n\t"
    "v_pk_mul_f32 v[206:207], v[206:207], v[66:67] op_sel_hi:[1,0]\n\t"
    "v_cvt_pk_bf16_f32 v200, v200, v201\n\t"
    "v_cvt_pk_bf16_f32 v201, v202, v203\n\t"
    "v_cvt_pk_bf16_f32 v202, v204, v205\n\t"
    "v_cvt_pk_bf16_f32 v203, v206, v207\n\t"
    "ds_write_b128 v229, v[200:203] offset:16384\n\t"
    "s_waitcnt lgkmcnt(2)\n\t"
    "s_add_u32 s67, s71, 0x4000\n\t"
    "buffer_store_dwordx4 v[128:131], v88, s[52:55], s67 offen offset:0 sc1\n\t"
    "buffer_store_dwordx4 v[136:139], v89, s[52:55], s67 offen offset:1024 sc1\n\t"
    "buffer_store_dwordx4 v[144:147], v90, s[52:55], s67 offen offset:2048 sc1\n\t"
    "buffer_store_dwordx4 v[152:155], v91, s[52:55], s67 offen offset:3072 sc1\n\t"
    "v_pk_mul_f32 v[208:209], v[208:209], v[66:67] op_sel_hi:[1,0]\n\t"
    "v_pk_mul_f32 v[210:211], v[210:211], v[66:67] op_sel_hi:[1,0]\n\t"
    "v_pk_mul_f32 v[212:213], v[212:213], v[66:67] op_sel_hi:[1,0]\n\t"
    "v_pk_mul_f32 v[214:215], v[214:215], v[66:67] op_sel_hi:[1,0]\n\t"
    "v_cvt_pk_bf16_f32 v208, v208, v209\n\t"
    "v_cvt_pk_bf16_f32 v209, v210, v211\n\t"
    "v_cvt_pk_bf16_f32 v210, v212, v213\n\t"
    "v_cvt_pk_bf16_f32 v211, v214, v215\n\t"
    "ds_write_b128 v230, v[208:211] offset:16384\n\t"
    "v_pk_mul_f32 v[216:217], v[216:217], v[66:67] op_sel_hi:[1,0]\n\t"
    "v_pk_mul_f32 v[218:219], v[218:219], v[66:67] op_sel_hi:[1,0]\n\t"
    "v_pk_mul_f32 v[220:221], v[220:221], v[66:67] op_sel_hi:[1,0]\n\t"
    "v_pk_mul_f32 v[222:223], v[222:223], v[66:67] op_sel_hi:[1,0]\n\t"
    "v_cvt_pk_bf16_f32 v216, v216, v217\n\t"
    "v_cvt_pk_bf16_f32 v217, v218, v219\n\t"
    "v_cvt_pk_bf16_f32 v218, v220, v221\n\t"
    "v_cvt_pk_bf16_f32 v219, v222, v223\n\t"
    "ds_write_b128 v231, v[216:219] offset:16384\n\t"
    "ds_read_b128 v[192:195], v85 offset:16384\n\t"
    "ds_read_b128 v[200:203], v85 offset:17408\n\t"
    "ds_read_b128 v[208:211], v85 offset:18432\n\t"
    "ds_read_b128 v[216:219], v85 offset:19456\n\t"
    "s_waitcnt lgkmcnt(0)\n\t"
    "s_add_u32 s67, s71, 0x5000\n\t"
    "buffer_store_dwordx4 v[192:195], v88, s[52:55], s67 offen offset:0 sc1\n\t"
    "buffer_store_dwordx4 v[200:203], v89, s[52:55], s67 offen offset:1024 sc1\n\t"
    "buffer_store_dwordx4 v[208:211], v90, s[52:55], s67 offen offset:2048 sc1\n\t"
    "buffer_store_dwordx4 v[216:219], v91, s[52:55], s67 offen offset:3072 sc1\n\t"
    "LF_%=:\n\t"
    "s_or_b32 %[flag], s72, s73\n\t"
    "s_mov_b64 %[qp], s[88:89]\n\t"
    "s_mov_b64 %[kp], s[90:91]\n\t"
    "s_mov_b64 %[vp], s[92:93]\n\t"
    "s_mov_b64 %[op], s[94:95]\n\t"
    "s_branch LEND_%=\n\t"
    "LHE_%=:\n\t"
    "v_add_u32_e32 v86, 0xffffd000, v86\n\t"
    "v_smfmac_f32_16x16x64_bf16 v[64:67], v[72:75], v[4:11], v93\n\t"
    "v_mov_b32_e32 v232, 0\n\t"
    "v_mov_b32_e32 v233, 0\n\t"
    "v_mov_b32_e32 v234, 0\n\t"
    "v_mov_b32_e32 v235, 0\n\t"
    "v_mov_b32_e32 v236, 0\n\t"
    "v_mov_b32_e32 v237, 0\n\t"
    "v_mov_b32_e32 v238, 0\n\t"
    "v_mov_b32_e32 v239, 0\n\t"
    "ds_read_b128 v[192:195], v86 offset:16384\n\t"
    "ds_read_b128 v[196:199], v86 offset:17408\n\t"
    "ds_read_b128 v[200:203], v86 offset:18432\n\t"
    "ds_read_b128 v[204:207], v86 offset:19456\n\t"
    "ds_read_b128 v[52:55], v86 offset:2048\n\t"
    "ds_read_b128 v[208:211], v86 offset:20480\n\t"
    "ds_read_b128 v[212:215], v86 offset:21504\n\t"
    "ds_read_b128 v[216:219], v86 offset:22528\n\t"
    "ds_read_b128 v[220:223], v86 offset:23552\n\t"
    "ds_read_b128 v[252:255], v86 offset:6144\n\t"
    "ds_read_b128 a[80:83], v86 offset:24576\n\t"
    "ds_read_b128 a[84:87], v86 offset:25600\n\t"
    "ds_read_b128 a[88:91], v86 offset:26624\n\t"
    "ds_read_b128 a[92:95], v86 offset:27648\n\t"
    "ds_read_b128 v[68:71], v86 offset:10240\n\t"
    "LH_%=:\n\t"
    "v_mfma_f32_16x16x32_bf16 v[24:27], a[96:99], a[16:19], v[36:39]\n\t"
    "v_exp_f32_e32 v23, v23\n\t"
    "v_mfma_f32_16x16x32_bf16 v[24:27], a[100:103], a[20:23], v[24:27]\n\t"
    "s_add_u32 s56, s56, 0x4000\n\t"
    "s_and_b32 s56, s56, 0x3ffff\n\t"
    "v_mfma_f32_16x16x32_bf16 v[24:27], a[104:107], a[24:27], v[24:27]\n\t"
    "v_exp_f32_e32 v22, v22\n\t"
    "v_mfma_f32_16x16x32_bf16 v[24:27], a[108:111], a[28:31], v[24:27]\n\t"
    "v_cvt_pk_bf16_f32 v23, v22, v23\n\t"
    "s_add_u32 s57, s57, s60\n\t"
    "v_mfma_f32_16x16x32_bf16 v[28:31], a[112:115], a[16:19], v[36:39]\n\t"
    "v_exp_f32_e32 v21, v21\n\t"
    "v_mfma_f32_16x16x32_bf16 v[28:31], a[116:119], a[20:23], v[28:31]\n\t"
    "s_waitcnt lgkmcnt(10)\n\t"
    "s_cmp_le_u32 s58, 12\n\t"
    "v_mfma_f32_16x16x32_bf16 v[28:31], a[120:123], a[24:27], v[28:31]\n\t"
    "v_exp_f32_e32 v20, v20\n\t"
    "v_mfma_f32_16x16x32_bf16 v[28:31], a[124:127], a[28:31], v[28:31]\n\t"
    "v_cvt_pk_bf16_f32 v22, v20, v21\n\t"
    "s_cselect_b32 s42, 0x40000, 0\n\t"
    "v_mfma_f32_16x16x32_bf16 a[224:227], a[128:131], v[236:239], a[224:227]\n\t"
    "v_exp_f32_e32 v19, v19\n\t"
    "v_mfma_f32_16x16x32_bf16 a[228:231], a[132:135], v[236:239], a[228:231]\n\t"
    "s_mov_b32 s46, s42\n\t"
    "s_mov_b32 m0, s57\n\t"
    "v_mfma_f32_16x16x32_bf16 a[232:235], a[136:139], v[236:239], a[232:235]\n\t"
    "v_exp_f32_e32 v18, v18\n\t"
    "v_mfma_f32_16x16x32_bf16 a[236:239], a[140:143], v[236:239], a[236:239]\n\t"
    "v_cvt_pk_bf16_f32 v21, v18, v19\n\t"
    "s_add_u32 s58, s58, 1\n\t"
    "v_mfma_f32_16x16x32_bf16 a[240:243], a[144:147], v[236:239], a[240:243]\n\t"
    "v_exp_f32_e32 v17, v17\n\t"
    "v_mfma_f32_16x16x32_bf16 a[244:247], a[148:151], v[236:239], a[244:247]\n\t"
    "buffer_load_dwordx4 v88, s[40:43], s56 offen offset:0 nt lds\n\t"
    "v_mfma_f32_16x16x32_bf16 a[248:251], a[152:155], v[236:239], a[248:251]\n\t"
    "v_exp_f32_e32 v16, v16\n\t"
    "v_mfma_f32_16x16x32_bf16 a[252:255], a[156:159], v[236:239], a[252:255]\n\t"
    "v_cvt_pk_bf16_f32 v20, v16, v17\n\t"
    "s_and_b32 s61, s58, 3\n\t"
    "v_smfmac_f32_16x16x64_bf16 v[64:67], v[72:75], v[232:239], v93\n\t"
    "buffer_load_dwordx4 v89, s[40:43], s56 offen offset:1024 nt lds\n\t"
    "v_mfma_f32_16x16x32_bf16 v[0:3], a[96:99], a[32:35], v[40:43]\n\t"
    "v_exp_f32_e32 v24, v24\n\t"
    "v_mfma_f32_16x16x32_bf16 v[0:3], a[100:103], a[36:39], v[0:3]\n\t"
    "buffer_load_dwordx4 v90, s[40:43], s56 offen offset:2048 nt lds\n\t"
    "v_mfma_f32_16x16x32_bf16 v[0:3], a[104:107], a[40:43], v[0:3]\n\t"
    "v_exp_f32_e32 v25, v25\n\t"
    "v_mfma_f32_16x16x32_bf16 v[0:3], a[108:111], a[44:47], v[0:3]\n\t"
    "v_cvt_pk_bf16_f32 v24, v24, v25\n\t"
    "s_cmp_eq_u32 s61, 3\n\t"
    "v_mfma_f32_16x16x32_bf16 v[4:7], a[112:115], a[32:35], v[40:43]\n\t"
    "v_exp_f32_e32 v26, v26\n\t"
    "v_mfma_f32_16x16x32_bf16 v[4:7], a[116:119], a[36:39], v[4:7]\n\t"
    "buffer_load_dwordx4 v91, s[40:43], s56 offen offset:3072 nt lds\n\t"
    "v_mfma_f32_16x16x32_bf16 v[4:7], a[120:123], a[40:43], v[4:7]\n\t"
    "v_exp_f32_e32 v27, v27\n\t"
    "v_mfma_f32_16x16x32_bf16 v[4:7], a[124:127], a[44:47], v[4:7]\n\t"
    "v_cvt_pk_bf16_f32 v25, v26, v27\n\t"
    "s_bitset1_b32 m0, 14\n\t"
    "v_mfma_f32_16x16x32_bf16 v[96:99], a[160:163], v[20:23], v[96:99]\n\t"
    "v_exp_f32_e32 v28, v28\n\t"
    "v_mfma_f32_16x16x32_bf16 v[100:103], a[164:167], v[20:23], v[100:103]\n\t"
    "ds_read_b64_tr_b16 a[128:129], v80 offset:0\n\t"
    "s_cselect_b32 s59, s63, 0x8000\n\t"
    "v_mfma_f32_16x16x32_bf16 v[104:107], a[168:171], v[20:23], v[104:107]\n\t"
    "v_exp_f32_e32 v29, v29\n\t"
    "v_mfma_f32_16x16x32_bf16 v[108:111], a[172:175], v[20:23], v[108:111]\n\t"
    "ds_read_b64_tr_b16 a[130:131], v80 offset:4096\n\t"
    "v_cvt_pk_bf16_f32 v26, v28, v29\n\t"
    "v_mfma_f32_16x16x32_bf16 v[112:115], a[176:179], v[20:23], v[112:115]\n\t"
    "v_exp_f32_e32 v30, v30\n\t"
    "v_mfma_f32_16x16x32_bf16 v[116:119], a[180:183], v[20:23], v[116:119]\n\t"
    "ds_read_b64_tr_b16 a[132:133], v80 offset:8\n\t"
    "s_cmp_eq_u32 s61, 1\n\t"
    "v_mfma_f32_16x16x32_bf16 v[120:123], a[184:187], v[20:23], v[120:123]\n\t"
    "v_exp_f32_e32 v31, v31\n\t"
    "v_mfma_f32_16x16x32_bf16 v[124:127], a[188:191], v[20:23], v[124:127]\n\t"
    "ds_read_b64_tr_b16 a[134:135], v80 offset:4104\n\t"
    "v_cvt_pk_bf16_f32 v27, v30, v31\n\t"
    "v_mfma_f32_16x16x32_bf16 v[8:11], a[96:99], a[48:51], v[44:47]\n\t"
    "v_exp_f32_e32 v7, v7\n\t"
    "v_mfma_f32_16x16x32_bf16 v[8:11], a[100:103], a[52:55], v[8:11]\n\t"
    "ds_read_b64_tr_b16 a[136:137], v81 offset:0\n\t"
    "s_cselect_b32 s60, s63, 0x8000\n\t"
    "v_mfma_f32_16x16x32_bf16 v[8:11], a[104:107], a[56:59], v[8:11]\n\t"
    "v_exp_f32_e32 v6, v6\n\t"
    "v_mfma_f32_16x16x32_bf16 v[8:11], a[108:111], a[60:63], v[8:11]\n\t"
    "ds_read_b64_tr_b16 a[138:139], v81 offset:4096\n\t"
    "v_cvt_pk_bf16_f32 v7, v6, v7\n\t"
    "v_mfma_f32_16x16x32_bf16 v[12:15], a[112:115], a[48:51], v[44:47]\n\t"
    "v_exp_f32_e32 v5, v5\n\t"
    "v_mfma_f32_16x16x32_bf16 v[12:15], a[116:119], a[52:55], v[12:15]\n\t"
    "ds_read_b64_tr_b16 a[140:141], v81 offset:8\n\t"
    "s_cmp_eq_u32 s58, 14\n\t"
    "v_mfma_f32_16x16x32_bf16 v[12:15], a[120:123], a[56:59], v[12:15]\n\t"
    "v_exp_f32_e32 v4, v4\n\t"
    "v_mfma_f32_16x16x32_bf16 v[12:15], a[124:127], a[60:63], v[12:15]\n\t"
    "ds_read_b64_tr_b16 a[142:143], v81 offset:4104\n\t"
    "v_cvt_pk_bf16_f32 v6, v4, v5\n\t"
    "v_mfma_f32_16x16x32_bf16 v[128:131], a[160:163], v[24:27], v[128:131]\n\t"
    "v_exp_f32_e32 v3, v3\n\t"
    "v_mfma_f32_16x16x32_bf16 v[132:135], a[164:167], v[24:27], v[132:135]\n\t"
    "ds_read_b64_tr_b16 a[144:145], v82 offset:0\n\t"
    "ds_read_b64_tr_b16 a[146:147], v82 offset:4096\n\t"
    "v_mfma_f32_16x16x32_bf16 v[136:139], a[168:171], v[24:27], v[136:139]\n\t"
    "v_exp_f32_e32 v2, v2\n\t"
    "v_mfma_f32_16x16x32_bf16 v[140:143], a[172:175], v[24:27], v[140:143]\n\t"
    "ds_read_b64_tr_b16 a[148:149], v82 offset:8\n\t"
    "v_cvt_pk_bf16_f32 v5, v2, v3\n\t"
    "v_mfma_f32_16x16x32_bf16 v[144:147], a[176:179], v[24:27], v[144:147]\n\t"
    "v_exp_f32_e32 v1, v1\n\t"
    "v_mfma_f32_16x16x32_bf16 v[148:151], a[180:183], v[24:27], v[148:151]\n\t"
    "ds_read_b64_tr_b16 a[150:151], v82 offset:4104\n\t"
    "ds_read_b64_tr_b16 a[152:153], v83 offset:0\n\t"
    "v_mfma_f32_16x16x32_bf16 v[152:155], a[184:187], v[24:27], v[152:155]\n\t"
    "v_exp_f32_e32 v0, v0\n\t"
    "v_mfma_f32_16x16x32_bf16 v[156:159], a[188:191], v[24:27], v[156:159]\n\t"
    "ds_read_b64_tr_b16 a[154:155], v83 offset:4096\n\t"
    "v_cvt_pk_bf16_f32 v4, v0, v1\n\t"
    "v_smfmac_f32_16x16x64_bf16 v[56:59], v[72:75], v[20:27], v93\n\t"
    "ds_read_b64_tr_b16 a[156:157], v83 offset:8\n\t"
    "ds_read_b64_tr_b16 a[158:159], v83 offset:4104\n\t"
    "v_mfma_f32_16x16x32_bf16 v[236:239], a[96:99], a[64:67], v[48:51]\n\t"
    "v_exp_f32_e32 v8, v8\n\t"
    "v_mfma_f32_16x16x32_bf16 v[236:239], a[100:103], a[68:71], v[236:239]\n\t"
    "ds_read_b128 a[96:99], v76 offset:0\n\t"
    "s_cselect_b32 s59, 0, s59\n\t"
    "v_mfma_f32_16x16x32_bf16 v[236:239], a[104:107], a[72:75], v[236:239]\n\t"
    "v_exp_f32_e32 v9, v9\n\t"
    "v_mfma_f32_16x16x32_bf16 v[236:239], a[108:111], a[76:79], v[236:239]\n\t"
    "ds_read_b128 a[100:103], v77 offset:0\n\t"
    "v_cvt_pk_bf16_f32 v8, v8, v9\n\t"
    "v_mfma_f32_16x16x32_bf16 v[240:243], a[112:115], a[64:67], v[48:51]\n\t"
    "v_exp_f32_e32 v10, v10\n\t"
    "v_mfma_f32_16x16x32_bf16 v[240:243], a[116:119], a[68:71], v[240:243]\n\t"
    "ds_read_b128 a[104:107], v78 offset:0\n\t"
    "ds_read_b128 a[108:111], v79 offset:0\n\t"
    "v_mfma_f32_16x16x32_bf16 v[240:243], a[120:123], a[72:75], v[240:243]\n\t"
    "v_exp_f32_e32 v11, v11\n\t"
    "v_mfma_f32_16x16x32_bf16 v[240:243], a[124:127], a[76:79], v[240:243]\n\t"
    "ds_read_b128 a[112:115], v76 offset:4096\n\t"
    "v_cvt_pk_bf16_f32 v9, v10, v11\n\t"
    "v_mfma_f32_16x16x32_bf16 v[160:163], a[160:163], v[4:7], v[160:163]\n\t"
    "v_exp_f32_e32 v12, v12\n\t"
    "v_mfma_f32_16x16x32_bf16 v[164:167], a[164:167], v[4:7], v[164:167]\n\t"
    "ds_read_b128 a[116:119], v77 offset:4096\n\t"
    "ds_read_b128 a[120:123], v78 offset:4096\n\t"
    "v_mfma_f32_16x16x32_bf16 v[168:171], a[168:171], v[4:7], v[168:171]\n\t"
    "v_exp_f32_e32 v13, v13\n\t"
    "v_mfma_f32_16x16x32_bf16 v[172:175], a[172:175], v[4:7], v[172:175]\n\t"
    "ds_read_b128 a[124:127], v79 offset:4096\n\t"
    "v_cvt_pk_bf16_f32 v10, v12, v13\n\t"
    "v_mfma_f32_16x16x32_bf16 v[176:179], a[176:179], v[4:7], v[176:179]\n\t"
    "v_exp_f32_e32 v14, v14\n\t"
    "v_mfma_f32_16x16x32_bf16 v[180:183], a[180:183], v[4:7], v[180:183]\n\t"
    "s_waitcnt lgkmcnt(2)\n\t"
    "v_mfma_f32_16x16x32_bf16 v[184:187], a[184:187], v[4:7], v[184:187]\n\t"
    "v_exp_f32_e32 v15, v15\n\t"
    "v_mfma_f32_16x16x32_bf16 v[188:191], a[188:191], v[4:7], v[188:191]\n\t"
    "v_cvt_pk_bf16_f32 v11, v14, v15\n\t"
    "v_mfma_f32_16x16x32_bf16 v[16:19], a[96:99], a[0:3], v[32:35]\n\t"
    "v_exp_f32_e32 v236, v236\n\t"
    "v_mfma_f32_16x16x32_bf16 v[16:19], a[100:103], a[4:7], v[16:19]\n\t"
    "buffer_load_dwordx4 v92, s[44:47], s56 offen offset:0 nt lds\n\t"
    "v_mfma_f32_16x16x32_bf16 v[16:19], a[104:107], a[8:11], v[16:19]\n\t"
    "v_exp_f32_e32 v237, v237\n\t"
    "v_mfma_f32_16x16x32_bf16 v[16:19], a[108:111], a[12:15], v[16:19]\n\t"
    "v_cvt_pk_bf16_f32 v232, v236, v237\n\t"
    "v_mfma_f32_16x16x32_bf16 v[20:23], a[112:115], a[0:3], v[32:35]\n\t"
    "v_exp_f32_e32 v238, v238\n\t"
    "v_mfma_f32_16x16x32_bf16 v[20:23], a[116:119], a[4:7], v[20:23]\n\t"
    "s_waitcnt lgkmcnt(0)\n\t"
    "v_mfma_f32_16x16x32_bf16 v[20:23], a[120:123], a[8:11], v[20:23]\n\t"
    "v_exp_f32_e32 v239, v239\n\t"
    "v_mfma_f32_16x16x32_bf16 v[20:23], a[124:127], a[12:15], v[20:23]\n\t"
    "v_cvt_pk_bf16_f32 v233, v238, v239\n\t"
    "v_mfma_f32_16x16x32_bf16 a[192:195], a[160:163], v[8:11], a[192:195]\n\t"
    "v_exp_f32_e32 v240, v240\n\t"
    "v_mfma_f32_16x16x32_bf16 a[196:199], a[164:167], v[8:11], a[196:199]\n\t"
    "buffer_load_dwordx4 v92, s[44:47], s56 offen offset:1024 nt lds\n\t"
    "v_mfma_f32_16x16x32_bf16 a[200:203], a[168:171], v[8:11], a[200:203]\n\t"
    "v_exp_f32_e32 v241, v241\n\t"
    "v_mfma_f32_16x16x32_bf16 a[204:207], a[172:175], v[8:11], a[204:207]\n\t"
    "v_cvt_pk_bf16_f32 v234, v240, v241\n\t"
    "v_mfma_f32_16x16x32_bf16 a[208:211], a[176:179], v[8:11], a[208:211]\n\t"
    "v_exp_f32_e32 v242, v242\n\t"
    "v_mfma_f32_16x16x32_bf16 a[212:215], a[180:183], v[8:11], a[212:215]\n\t"
    "buffer_load_dwordx4 v92, s[44:47], s56 offen offset:2048 nt lds\n\t"
    "v_mfma_f32_16x16x32_bf16 a[216:219], a[184:187], v[8:11], a[216:219]\n\t"
    "v_exp_f32_e32 v243, v243\n\t"
    "v_mfma_f32_16x16x32_bf16 a[220:223], a[188:191], v[8:11], a[220:223]\n\t"
    "v_cvt_pk_bf16_f32 v235, v242, v243\n\t"
    "v_smfmac_f32_16x16x64_bf16 v[60:63], v[72:75], v[4:11], v93\n\t"
    "buffer_load_dwordx4 v92, s[44:47], s56 offen offset:3072 nt lds\n\t"
    "v_mfma_f32_16x16x32_bf16 v[244:247], a[96:99], v[192:195], v[52:55]\n\t"
    "v_exp_f32_e32 v23, v23\n\t"
    "v_mfma_f32_16x16x32_bf16 v[244:247], a[100:103], v[196:199], v[244:247]\n\t"
    "v_exp_f32_e32 v22, v22\n\t"
    "v_mfma_f32_16x16x32_bf16 v[244:247], a[104:107], v[200:203], v[244:247]\n\t"
    "v_cvt_pk_bf16_f32 v23, v22, v23\n\t"
    "v_mfma_f32_16x16x32_bf16 v[244:247], a[108:111], v[204:207], v[244:247]\n\t"
    "v_exp_f32_e32 v21, v21\n\t"
    "v_mfma_f32_16x16x32_bf16 v[248:251], a[112:115], v[192:195], v[52:55]\n\t"
    "v_exp_f32_e32 v20, v20\n\t"
    "v_mfma_f32_16x16x32_bf16 v[248:251], a[116:119], v[196:199], v[248:251]\n\t"
    "v_cvt_pk_bf16_f32 v22, v20, v21\n\t"
    "v_mfma_f32_16x16x32_bf16 v[248:251], a[120:123], v[200:203], v[248:251]\n\t"
    "v_exp_f32_e32 v19, v19\n\t"
    "v_mfma_f32_16x16x32_bf16 v[248:251], a[124:127], v[204:207], v[248:251]\n\t"
    "v_exp_f32_e32 v18, v18\n\t"
    "v_mfma_f32_16x16x32_bf16 a[224:227], a[160:163], v[232:235], a[224:227]\n\t"
    "v_cvt_pk_bf16_f32 v21, v18, v19\n\t"
    "v_mfma_f32_16x16x32_bf16 a[228:231], a[164:167], v[232:235], a[228:231]\n\t"
    "v_exp_f32_e32 v17, v17\n\t"
    "v_mfma_f32_16x16x32_bf16 a[232:235], a[168:171], v[232:235], a[232:235]\n\t"
    "v_exp_f32_e32 v16, v16\n\t"
    "v_mfma_f32_16x16x32_bf16 a[236:239], a[172:175], v[232:235], a[236:239]\n\t"
    "ds_read_b64_tr_b16 a[160:161], v80 offset:8192\n\t"
    "v_cvt_pk_bf16_f32 v20, v16, v17\n\t"
    "v_mfma_f32_16x16x32_bf16 a[240:243], a[176:179], v[232:235], a[240:243]\n\t"
    "v_exp_f32_e32 v251, v251\n\t"
    "v_mfma_f32_16x16x32_bf16 a[244:247], a[180:183], v[232:235], a[244:247]\n\t"
    "v_exp_f32_e32 v250, v250\n\t"
    "v_mfma_f32_16x16x32_bf16 a[248:251], a[184:187], v[232:235], a[248:251]\n\t"
    "ds_read_b64_tr_b16 a[162:163], v80 offset:12288\n\t"
    "v_cvt_pk_bf16_f32 v251, v250, v251\n\t"
    "v_mfma_f32_16x16x32_bf16 a[252:255], a[188:191], v[232:235], a[252:255]\n\t"
    "v_exp_f32_e32 v249, v249\n\t"
    "v_mfma_f32_16x16x32_bf16 v[24:27], a[96:99], a[16:19], v[36:39]\n\t"
    "v_exp_f32_e32 v248, v248\n\t"
    "v_mfma_f32_16x16x32_bf16 v[24:27], a[100:103], a[20:23], v[24:27]\n\t"
    "ds_read_b64_tr_b16 a[164:165], v80 offset:8200\n\t"
    "v_cvt_pk_bf16_f32 v250, v248, v249\n\t"
    "v_mfma_f32_16x16x32_bf16 v[24:27], a[104:107], a[24:27], v[24:27]\n\t"
    "v_exp_f32_e32 v247, v247\n\t"
    "v_mfma_f32_16x16x32_bf16 v[24:27], a[108:111], a[28:31], v[24:27]\n\t"
    "v_exp_f32_e32 v246, v246\n\t"
    "v_mfma_f32_16x16x32_bf16 v[28:31], a[112:115], a[16:19], v[36:39]\n\t"
    "ds_read_b64_tr_b16 a[166:167], v80 offset:12296\n\t"
    "v_cvt_pk_bf16_f32 v249, v246, v247\n\t"
    "v_mfma_f32_16x16x32_bf16 v[28:31], a[116:119], a[20:23], v[28:31]\n\t"
    "v_exp_f32_e32 v245, v245\n\t"
    "v_mfma_f32_16x16x32_bf16 v[28:31], a[120:123], a[24:27], v[28:31]\n\t"
    "v_exp_f32_e32 v244, v244\n\t"
    "v_mfma_f32_16x16x32_bf16 v[28:31], a[124:127], a[28:31], v[28:31]\n\t"
    "ds_read_b64_tr_b16 a[168:169], v81 offset:8192\n\t"
    "v_cvt_pk_bf16_f32 v248, v244, v245\n\t"
    "v_mfma_f32_16x16x32_bf16 v[224:227], a[96:99], v[208:211], v[252:255]\n\t"
    "v_exp_f32_e32 v24, v24\n\t"
    "v_mfma_f32_16x16x32_bf16 v[224:227], a[100:103], v[212:215], v[224:227]\n\t"
    "v_exp_f32_e32 v25, v25\n\t"
    "v_mfma_f32_16x16x32_bf16 v[224:227], a[104:107], v[216:219], v[224:227]\n\t"
    "ds_write_b128 v86, v[248:251] offset:0\n\t"
    "v_cvt_pk_bf16_f32 v24, v24, v25\n\t"
    "v_mfma_f32_16x16x32_bf16 v[224:227], a[108:111], v[220:223], v[224:227]\n\t"
    "v_exp_f32_e32 v26, v26\n\t"
    "v_mfma_f32_16x16x32_bf16 v[228:231], a[112:115], v[208:211], v[252:255]\n\t"
    "v_exp_f32_e32 v27, v27\n\t"
    "v_mfma_f32_16x16x32_bf16 v[228:231], a[116:119], v[212:215], v[228:231]\n\t"
    "ds_read_b64_tr_b16 a[170:171], v81 offset:12288\n\t"
    "v_cvt_pk_bf16_f32 v25, v26, v27\n\t"
    "v_mfma_f32_16x16x32_bf16 v[228:231], a[120:123], v[216:219], v[228:231]\n\t"
    "v_exp_f32_e32 v28, v28\n\t"
    "v_mfma_f32_16x16x32_bf16 v[228:231], a[124:127], v[220:223], v[228:231]\n\t"
    "v_exp_f32_e32 v29, v29\n\t"
    "v_mfma_f32_16x16x32_bf16 v[0:3], a[96:99], a[32:35], v[40:43]\n\t"
    "ds_read_b64_tr_b16 a[172:173], v81 offset:8200\n\t"
    "v_cvt_pk_bf16_f32 v26, v28, v29\n\t"
    "v_mfma_f32_16x16x32_bf16 v[0:3], a[100:103], a[36:39], v[0:3]\n\t"
    "v_exp_f32_e32 v30, v30\n\t"
    "v_mfma_f32_16x16x32_bf16 v[0:3], a[104:107], a[40:43], v[0:3]\n\t"
    "v_exp_f32_e32 v31, v31\n\t"
    "v_mfma_f32_16x16x32_bf16 v[0:3], a[108:111], a[44:47], v[0:3]\n\t"
    "ds_read_b64_tr_b16 a[174:175], v81 offset:12296\n\t"
    "v_cvt_pk_bf16_f32 v27, v30, v31\n\t"
    "v_mfma_f32_16x16x32_bf16 v[4:7], a[112:115], a[32:35], v[40:43]\n\t"
    "v_exp_f32_e32 v231, v231\n\t"
    "v_mfma_f32_16x16x32_bf16 v[4:7], a[116:119], a[36:39], v[4:7]\n\t"
    "v_exp_f32_e32 v230, v230\n\t"
    "v_mfma_f32_16x16x32_bf16 v[4:7], a[120:123], a[40:43], v[4:7]\n\t"
    "ds_read_b64_tr_b16 a[176:177], v82 offset:8192\n\t"
    "v_cvt_pk_bf16_f32 v231, v230, v231\n\t"
    "v_mfma_f32_16x16x32_bf16 v[4:7], a[124:127], a[44:47], v[4:7]\n\t"
    "v_exp_f32_e32 v229, v229\n\t"
    "v_mfma_f32_16x16x32_bf16 v[96:99], a[128:131], v[20:23], v[96:99]\n\t"
    "v_exp_f32_e32 v228, v228\n\t"
    "v_mfma_f32_16x16x32_bf16 v[100:103], a[132:135], v[20:23], v[100:103]\n\t"
    "ds_read_b64_tr_b16 a[178:179], v82 offset:12288\n\t"
    "v_cvt_pk_bf16_f32 v230, v228, v229\n\t"
    "v_mfma_f32_16x16x32_bf16 v[104:107], a[136:139], v[20:23], v[104:107]\n\t"
    "v_exp_f32_e32 v227, v227\n\t"
    "v_mfma_f32_16x16x32_bf16 v[108:111], a[140:143], v[20:23], v[108:111]\n\t"
    "v_exp_f32_e32 v226, v226\n\t"
    "v_mfma_f32_16x16x32_bf16 v[112:115], a[144:147], v[20:23], v[112:115]\n\t"
    "ds_read_b64_tr_b16 a[180:181], v82 offset:8200\n\t"
    "v_cvt_pk_bf16_f32 v229, v226, v227\n\t"
    "v_mfma_f32_16x16x32_bf16 v[116:119], a[148:151], v[20:23], v[116:119]\n\t"
    "v_exp_f32_e32 v225, v225\n\t"
    "v_mfma_f32_16x16x32_bf16 v[120:123], a[152:155], v[20:23], v[120:123]\n\t"
    "v_exp_f32_e32 v224, v224\n\t"
    "v_mfma_f32_16x16x32_bf16 v[124:127], a[156:159], v[20:23], v[124:127]\n\t"
    "ds_read_b64_tr_b16 a[182:183], v82 offset:12296\n\t"
    "v_cvt_pk_bf16_f32 v228, v224, v225\n\t"
    "v_mfma_f32_16x16x32_bf16 v[244:247], a[96:99], a[80:83], v[68:71]\n\t"
    "v_exp_f32_e32 v7, v7\n\t"
    "v_mfma_f32_16x16x32_bf16 v[244:247], a[100:103], a[84:87], v[244:247]\n\t"
    "v_exp_f32_e32 v6, v6\n\t"
    "v_mfma_f32_16x16x32_bf16 v[244:247], a[104:107], a[88:91], v[244:247]\n\t"
    "ds_read_b64_tr_b16 a[184:185], v83 offset:8192\n\t"
    "v_cvt_pk_bf16_f32 v7, v6, v7\n\t"
    "v_mfma_f32_16x16x32_bf16 v[244:247], a[108:111], a[92:95], v[244:247]\n\t"
    "v_exp_f32_e32 v5, v5\n\t"
    "v_mfma_f32_16x16x32_bf16 v[248:251], a[112:115], a[80:83], v[68:71]\n\t"
    "v_exp_f32_e32 v4, v4\n\t"
    "v_mfma_f32_16x16x32_bf16 v[248:251], a[116:119], a[84:87], v[248:251]\n\t"
    "ds_read_b64_tr_b16 a[186:187], v83 offset:12288\n\t"
    "v_cvt_pk_bf16_f32 v6, v4, v5\n\t"
    "v_mfma_f32_16x16x32_bf16 v[248:251], a[120:123], a[88:91], v[248:251]\n\t"
    "v_exp_f32_e32 v3, v3\n\t"
    "v_mfma_f32_16x16x32_bf16 v[248:251], a[124:127], a[92:95], v[248:251]\n\t"
    "v_exp_f32_e32 v2, v2\n\t"
    "v_mfma_f32_16x16x32_bf16 v[128:131], a[128:131], v[24:27], v[128:131]\n\t"
    "ds_read_b64_tr_b16 a[188:189], v83 offset:8200\n\t"
    "v_cvt_pk_bf16_f32 v5, v2, v3\n\t"
    "v_mfma_f32_16x16x32_bf16 v[132:135], a[132:135], v[24:27], v[132:135]\n\t"
    "v_exp_f32_e32 v1, v1\n\t"
    "v_mfma_f32_16x16x32_bf16 v[136:139], a[136:139], v[24:27], v[136:139]\n\t"
    "v_exp_f32_e32 v0, v0\n\t"
    "v_mfma_f32_16x16x32_bf16 v[140:143], a[140:143], v[24:27], v[140:143]\n\t"
    "ds_read_b64_tr_b16 a[190:191], v83 offset:12296\n\t"
    "v_cvt_pk_bf16_f32 v4, v0, v1\n\t"
    "v_mfma_f32_16x16x32_bf16 v[144:147], a[144:147], v[24:27], v[144:147]\n\t"
    "v_exp_f32_e32 v251, v251\n\t"
    "v_mfma_f32_16x16x32_bf16 v[148:151], a[148:151], v[24:27], v[148:151]\n\t"
    "v_exp_f32_e32 v250, v250\n\t"
    "v_mfma_f32_16x16x32_bf16 v[152:155], a[152:155], v[24:27], v[152:155]\n\t"
    "v_cvt_pk_bf16_f32 v251, v250, v251\n\t"
    "v_mfma_f32_16x16x32_bf16 v[156:159], a[156:159], v[24:27], v[156:159]\n\t"
    "v_exp_f32_e32 v249, v249\n\t"
    "v_smfmac_f32_16x16x64_bf16 v[56:59], v[72:75], v[20:27], v93\n\t"
    "ds_write_b128 v86, v[228:231] offset:4096\n\t"
    "v_mfma_f32_16x16x32_bf16 v[8:11], a[96:99], a[48:51], v[44:47]\n\t"
    "v_exp_f32_e32 v248, v248\n\t"
    "v_mfma_f32_16x16x32_bf16 v[8:11], a[100:103], a[52:55], v[8:11]\n\t"
    "v_cvt_pk_bf16_f32 v250, v248, v249\n\t"
    "v_mfma_f32_16x16x32_bf16 v[8:11], a[104:107], a[56:59], v[8:11]\n\t"
    "v_exp_f32_e32 v247, v247\n\t"
    "v_mfma_f32_16x16x32_bf16 v[8:11], a[108:111], a[60:63], v[8:11]\n\t"
    "v_exp_f32_e32 v246, v246\n\t"
    "v_mfma_f32_16x16x32_bf16 v[12:15], a[112:115], a[48:51], v[44:47]\n\t"
    "v_cvt_pk_bf16_f32 v249, v246, v247\n\t"
    "v_mfma_f32_16x16x32_bf16 v[12:15], a[116:119], a[52:55], v[12:15]\n\t"
    "v_exp_f32_e32 v245, v245\n\t"
    "v_mfma_f32_16x16x32_bf16 v[12:15], a[120:123], a[56:59], v[12:15]\n\t"
    "v_exp_f32_e32 v244, v244\n\t"
    "v_mfma_f32_16x16x32_bf16 v[12:15], a[124:127], a[60:63], v[12:15]\n\t"
    "v_cvt_pk_bf16_f32 v248, v244, v245\n\t"
    "v_mfma_f32_16x16x32_bf16 v[236:239], a[96:99], a[64:67], v[48:51]\n\t"
    "v_exp_f32_e32 v8, v8\n\t"
    "v_mfma_f32_16x16x32_bf16 v[236:239], a[100:103], a[68:71], v[236:239]\n\t"
    "ds_read_b128 a[96:99], v76 offset:8192\n\t"
    "ds_write_b128 v86, v[248:251] offset:8192\n\t"
    "v_mfma_f32_16x16x32_bf16 v[236:239], a[104:107], a[72:75], v[236:239]\n\t"
    "v_exp_f32_e32 v9, v9\n\t"
    "v_mfma_f32_16x16x32_bf16 v[236:239], a[108:111], a[76:79], v[236:239]\n\t"
    "ds_read_b128 a[100:103], v77 offset:8192\n\t"
    "v_cvt_pk_bf16_f32 v8, v8, v9\n\t"
    "v_mfma_f32_16x16x32_bf16 v[240:243], a[112:115], a[64:67], v[48:51]\n\t"
    "v_exp_f32_e32 v10, v10\n\t"
    "v_mfma_f32_16x16x32_bf16 v[240:243], a[116:119], a[68:71], v[240:243]\n\t"
    "ds_read_b128 a[104:107], v78 offset:8192\n\t"
    "ds_read_b128 a[108:111], v79 offset:8192\n\t"
    "v_mfma_f32_16x16x32_bf16 v[240:243], a[120:123], a[72:75], v[240:243]\n\t"
    "v_exp_f32_e32 v11, v11\n\t"
    "v_mfma_f32_16x16x32_bf16 v[240:243], a[124:127], a[76:79], v[240:243]\n\t"
    "ds_read_b128 a[112:115], v76 offset:12288\n\t"
    "v_cvt_pk_bf16_f32 v9, v10, v11\n\t"
    "v_mfma_f32_16x16x32_bf16 v[160:163], a[128:131], v[4:7], v[160:163]\n\t"
    "v_exp_f32_e32 v12, v12\n\t"
    "v_mfma_f32_16x16x32_bf16 v[164:167], a[132:135], v[4:7], v[164:167]\n\t"
    "ds_read_b128 a[116:119], v77 offset:12288\n\t"
    "ds_read_b128 a[120:123], v78 offset:12288\n\t"
    "v_mfma_f32_16x16x32_bf16 v[168:171], a[136:139], v[4:7], v[168:171]\n\t"
    "v_exp_f32_e32 v13, v13\n\t"
    "v_mfma_f32_16x16x32_bf16 v[172:175], a[140:143], v[4:7], v[172:175]\n\t"
    "ds_read_b128 a[124:127], v79 offset:12288\n\t"
    "v_cvt_pk_bf16_f32 v10, v12, v13\n\t"
    "v_mfma_f32_16x16x32_bf16 v[176:179], a[144:147], v[4:7], v[176:179]\n\t"
    "v_exp_f32_e32 v14, v14\n\t"
    "v_mfma_f32_16x16x32_bf16 v[180:183], a[148:151], v[4:7], v[180:183]\n\t"
    "s_waitcnt lgkmcnt(2)\n\t"
    "v_mfma_f32_16x16x32_bf16 v[184:187], a[152:155], v[4:7], v[184:187]\n\t"
    "v_exp_f32_e32 v15, v15\n\t"
    "v_mfma_f32_16x16x32_bf16 v[188:191], a[156:159], v[4:7], v[188:191]\n\t"
    "v_cvt_pk_bf16_f32 v11, v14, v15\n\t"
    "v_mfma_f32_16x16x32_bf16 v[16:19], a[96:99], a[0:3], v[32:35]\n\t"
    "v_exp_f32_e32 v236, v236\n\t"
    "v_mfma_f32_16x16x32_bf16 v[16:19], a[100:103], a[4:7], v[16:19]\n\t"
    "v_exp_f32_e32 v237, v237\n\t"
    "v_mfma_f32_16x16x32_bf16 v[16:19], a[104:107], a[8:11], v[16:19]\n\t"
    "v_cvt_pk_bf16_f32 v236, v236, v237\n\t"
    "s_waitcnt lgkmcnt(7)\n\t"
    "v_mfma_f32_16x16x32_bf16 v[16:19], a[108:111], a[12:15], v[16:19]\n\t"
    "v_exp_f32_e32 v238, v238\n\t"
    "v_mfma_f32_16x16x32_bf16 v[20:23], a[112:115], a[0:3], v[32:35]\n\t"
    "v_exp_f32_e32 v239, v239\n\t"
    "v_mfma_f32_16x16x32_bf16 v[20:23], a[116:119], a[4:7], v[20:23]\n\t"
    "s_waitcnt lgkmcnt(0)\n\t"
    "v_cvt_pk_bf16_f32 v237, v238, v239\n\t"
    "v_mfma_f32_16x16x32_bf16 v[20:23], a[120:123], a[8:11], v[20:23]\n\t"
    "v_exp_f32_e32 v240, v240\n\t"
    "v_mfma_f32_16x16x32_bf16 v[20:23], a[124:127], a[12:15], v[20:23]\n\t"
    "v_exp_f32_e32 v241, v241\n\t"
    "v_mfma_f32_16x16x32_bf16 a[192:195], a[128:131], v[8:11], a[192:195]\n\t"
    "v_cvt_pk_bf16_f32 v238, v240, v241\n\t"
    "v_add_u32_e32 v76, s59, v76\n\t"
    "v_mfma_f32_16x16x32_bf16 a[196:199], a[132:135], v[8:11], a[196:199]\n\t"
    "v_exp_f32_e32 v242, v242\n\t"
    "v_mfma_f32_16x16x32_bf16 a[200:203], a[136:139], v[8:11], a[200:203]\n\t"
    "v_exp_f32_e32 v243, v243\n\t"
    "v_mfma_f32_16x16x32_bf16 a[204:207], a[140:143], v[8:11], a[204:207]\n\t"
    "v_cvt_pk_bf16_f32 v239, v242, v243\n\t"
    "v_add_u32_e32 v77, s59, v77\n\t"
    "v_mfma_f32_16x16x32_bf16 a[208:211], a[144:147], v[8:11], a[208:211]\n\t"
    "v_add_u32_e32 v78, s59, v78\n\t"
    "v_add_u32_e32 v79, s59, v79\n\t"
    "v_mfma_f32_16x16x32_bf16 a[212:215], a[148:151], v[8:11], a[212:215]\n\t"
    "v_add_u32_e32 v80, s59, v80\n\t"
    "v_add_u32_e32 v81, s59, v81\n\t"
    "v_mfma_f32_16x16x32_bf16 a[216:219], a[152:155], v[8:11], a[216:219]\n\t"
    "s_waitcnt vmcnt(8)\n\t"
    "s_barrier\n\t"
    "v_mfma_f32_16x16x32_bf16 a[220:223], a[156:159], v[8:11], a[220:223]\n\t"
    "v_xor_b32_e32 v86, 0x400, v86\n\t"
    "v_add_u32_e32 v82, s59, v82\n\t"
    "v_smfmac_f32_16x16x64_bf16 v[60:63], v[72:75], v[4:11], v93\n\t"
    "v_add_u32_e32 v83, s59, v83\n\t"
    "s_cbranch_scc0 LH_%=\n\t"
    "s_nop 1\n\t"
    "v_smfmac_f32_16x16x64_bf16 v[64:67], v[72:75], v[232:239], v93\n\t"
    "v_mfma_f32_16x16x32_bf16 a[224:227], a[128:131], v[236:239], a[224:227]\n\t"
    "v_mfma_f32_16x16x32_bf16 a[228:231], a[132:135], v[236:239], a[228:231]\n\t"
    "v_mov_b32_e32 v52, s74\n\t"
    "v_mov_b32_e32 v53, s74\n\t"
    "v_mfma_f32_16x16x32_bf16 a[232:235], a[136:139], v[236:239], a[232:235]\n\t"
    "v_mov_b32_e32 v54, s74\n\t"
    "v_mov_b32_e32 v55, s74\n\t"
    "v_mfma_f32_16x16x32_bf16 a[236:239], a[140:143], v[236:239], a[236:239]\n\t"
    "v_mov_b32_e32 v4, 0\n\t"
    "v_mov_b32_e32 v5, 0\n\t"
    "v_mfma_f32_16x16x32_bf16 a[240:243], a[144:147], v[236:239], a[240:243]\n\t"
    "v_mov_b32_e32 v6, 0\n\t"
    "v_mov_b32_e32 v7, 0\n\t"
    "v_mfma_f32_16x16x32_bf16 a[244:247], a[148:151], v[236:239], a[244:247]\n\t"
    "v_mov_b32_e32 v8, 0\n\t"
    "v_mov_b32_e32 v9, 0\n\t"
    "v_mfma_f32_16x16x32_bf16 a[248:251], a[152:155], v[236:239], a[248:251]\n\t"
    "v_mov_b32_e32 v10, 0\n\t"
    "v_mov_b32_e32 v11, 0\n\t"
    "v_mfma_f32_16x16x32_bf16 a[252:255], a[156:159], v[236:239], a[252:255]\n\t"
    "v_add_f32_e32 v64, v66, v64\n\t"
    "v_add_f32_e32 v65, v67, v65\n\t"
    "v_mov_b32_e32 v66, 0\n\t"
    "v_mov_b32_e32 v67, 0\n\t"
    "v_mbcnt_lo_u32_b32 v240, -1, 0\n\t"
    "v_mbcnt_hi_u32_b32 v240, -1, v240\n\t"
    "v_and_b32_e32 v240, 3, v240\n\t"
    "v_mov_b32_e32 v241, 0x3f803f80\n\t"
    "v_cmp_eq_u32_e32 vcc, 0, v240\n\t"
    "s_nop 1\n\t"
    "v_cndmask_b32_e64 v232, 0, v241, vcc\n\t"
    "v_cndmask_b32_e64 v233, 0, v241, vcc\n\t"
    "v_cndmask_b32_e64 v234, 0, v241, vcc\n\t"
    "v_cndmask_b32_e64 v235, 0, v241, vcc\n\t"
    "v_cmp_eq_u32_e32 vcc, 2, v240\n\t"
    "s_nop 1\n\t"
    "v_cndmask_b32_e64 v236, 0, v241, vcc\n\t"
    "v_cndmask_b32_e64 v237, 0, v241, vcc\n\t"
    "v_cndmask_b32_e64 v238, 0, v241, vcc\n\t"
    "v_cndmask_b32_e64 v239, 0, v241, vcc\n\t"
    "s_sub_u32 s67, s69, 0x10000\n\t"
    "s_add_u32 s68, s69, 0x10000\n\t"
    "v_add_u32_e32 v224, s67, v76\n\t"
    "v_add_u32_e32 v228, s68, v76\n\t"
    "v_add_u32_e32 v225, s67, v77\n\t"
    "v_add_u32_e32 v229, s68, v77\n\t"
    "v_add_u32_e32 v226, s67, v78\n\t"
    "v_add_u32_e32 v230, s68, v78\n\t"
    "v_add_u32_e32 v227, s67, v79\n\t"
    "v_add_u32_e32 v231, s68, v79\n\t"
    "s_branch LT_%=\n\t"
    "LS5_%=:\n\t"
    "s_waitcnt lgkmcnt(0)\n\t"
    "s_add_u32 s67, s71, 0x4000\n\t"
    "buffer_store_dwordx4 v[128:131], v88, s[52:55], s67 offen offset:0 sc1\n\t"
    "buffer_store_dwordx4 v[136:139], v89, s[52:55], s67 offen offset:1024 sc1\n\t"
    "buffer_store_dwordx4 v[144:147], v90, s[52:55], s67 offen offset:2048 sc1\n\t"
    "buffer_store_dwordx4 v[152:155], v91, s[52:55], s67 offen offset:3072 sc1\n\t"
    "s_branch LF_%=\n\t"
    "LEND_%=:\n\t"
    "s_mov_b32 m0, s39\n\t"
    : [flag] "=&s"(flag), [qp] "=&s"(Qh), [kp] "=&s"(Kh), [vp] "=&s"(Vh), [op] "=&s"(Oh)
    : [karg] "s"(__builtin_amdgcn_kernarg_segment_ptr()), [bh] "s"(bh), [wid] "s"(wid), [lds] "s"(ldsb), [rot] "s"(rot)
    : "s39", "v0", "v1", "v2", "v3", "v4", "v5", "v6", "v7", "v8", "v9", "v10", "v11", "v12", "v13", "v14", "v15", "v16", "v17", "v18", "v19", "v20", "v21", "v22", "v23", "v24", "v25", "v26", "v27", "v28", "v29", "v30", "v31", "v32", "v33", "v34", "v35", "v36", "v37", "v38", "v39", "v40", "v41", "v42", "v43", "v44", "v45", "v46", "v47", "v48", "v49", "v50", "v51", "v52", "v53", "v54", "v55", "v56", "v57", "v58", "v59", "v60", "v61", "v62", "v63", "v64", "v65", "v66", "v67", "v68", "v69", "v70", "v71", "v72", "v73", "v74", "v75", "v76", "v77", "v78", "v79", "v80", "v81", "v82", "v83", "v84", "v85", "v86", "v87", "v88", "v89", "v90", "v91", "v92", "v93", "v94", "v95", "v96", "v97", "v98", "v99", "v100", "v101", "v102", "v103", "v104", "v105", "v106", "v107", "v108", "v109", "v110", "v111", "v112", "v113", "v114", "v115", "v116", "v117", "v118", "v119", "v120", "v121", "v122", "v123", "v124", "v125", "v126", "v127", "v128", "v129", "v130", "v131", "v132", "v133", "v134", "v135", "v136", "v137", "v138", "v139", "v140", "v141", "v142", "v143", "v144", "v145", "v146", "v147", "v148", "v149", "v150", "v151", "v152", "v153", "v154", "v155", "v156", "v157", "v158", "v159", "v160", "v161", "v162", "v163", "v164", "v165", "v166", "v167", "v168", "v169", "v170", "v171", "v172", "v173", "v174", "v175", "v176", "v177", "v178", "v179", "v180", "v181", "v182", "v183", "v184", "v185", "v186", "v187", "v188", "v189", "v190", "v191", "v192", "v193", "v194", "v195", "v196", "v197", "v198", "v199", "v200", "v201", "v202", "v203", "v204", "v205", "v206", "v207", "v208", "v209", "v210", "v211", "v212", "v213", "v214", "v215", "v216", "v217", "v218", "v219", "v220", "v221", "v222", "v223", "v224", "v225", "v226", "v227", "v228", "v229", "v230", "v231", "v232", "v233", "v234", "v235", "v236", "v237", "v238", "v239", "v240", "v241", "v242", "v243", "v244", "v245", "v246", "v247", "v248", "v249", "v250", "v251", "v252", "v253", "v254", "v255", "a0", "a1", "a2", "a3", "a4", "a5", "a6", "a7", "a8", "a9", "a10", "a11", "a12", "a13", "a14", "a15", "a16", "a17", "a18", "a19", "a20", "a21", "a22", "a23", "a24", "a25", "a26", "a27", "a28", "a29", "a30", "a31", "a32", "a33", "a34", "a35", "a36", "a37", "a38", "a39", "a40", "a41", "a42", "a43", "a44", "a45", "a46", "a47", "a48", "a49", "a50", "a51", "a52", "a53", "a54", "a55", "a56", "a57", "a58", "a59", "a60", "a61", "a62", "a63", "a64", "a65", "a66", "a67", "a68", "a69", "a70", "a71", "a72", "a73", "a74", "a75", "a76", "a77", "a78", "a79", "a80", "a81", "a82", "a83", "a84", "a85", "a86", "a87", "a88", "a89", "a90", "a91", "a92", "a93", "a94", "a95", "a96", "a97", "a98", "a99", "a100", "a101", "a102", "a103", "a104", "a105", "a106", "a107", "a108", "a109", "a110", "a111", "a112", "a113", "a114", "a115", "a116", "a117", "a118", "a119", "a120", "a121", "a122", "a123", "a124", "a125", "a126", "a127", "a128", "a129", "a130", "a131", "a132", "a133", "a134", "a135", "a136", "a137", "a138", "a139", "a140", "a141", "a142", "a143", "a144", "a145", "a146", "a147", "a148", "a149", "a150", "a151", "a152", "a153", "a154", "a155", "a156", "a157", "a158", "a159", "a160", "a161", "a162", "a163", "a164", "a165", "a166", "a167", "a168", "a169", "a170", "a171", "a172", "a173", "a174", "a175", "a176", "a177", "a178", "a179", "a180", "a181", "a182", "a183", "a184", "a185", "a186", "a187", "a188", "a189", "a190", "a191", "a192", "a193", "a194", "a195", "a196", "a197", "a198", "a199", "a200", "a201", "a202", "a203", "a204", "a205", "a206", "a207", "a208", "a209", "a210", "a211", "a212", "a213", "a214", "a215", "a216", "a217", "a218", "a219", "a220", "a221", "a222", "a223", "a224", "a225", "a226", "a227", "a228", "a229", "a230", "a231", "a232", "a233", "a234", "a235", "a236", "a237", "a238", "a239", "a240", "a241", "a242", "a243", "a244", "a245", "a246", "a247", "a248", "a249", "a250", "a251", "a252", "a253", "a254", "a255", "s40", "s41", "s42", "s43", "s44", "s45", "s46", "s47", "s48", "s49", "s50", "s51", "s52", "s53", "s54", "s55", "s56", "s57", "s58", "s59", "s60", "s61", "s62", "s63", "s64", "s65", "s66", "s67", "s68", "s69", "s70", "s71", "s72", "s73", "s74", "s75", "s76", "s77", "s78", "s79", "s80", "s81", "s82", "s83", "s84", "s85", "s86", "s87", "s88", "s89", "s90", "s91", "s92", "s93", "s94", "s95", "vcc", "scc", "memory");
  if (flag != 0u) {
    asm volatile("s_waitcnt vmcnt(0)" ::: "memory");
    exact_path(Qh, Kh, Vh, Oh, (lds_ptr)lds + 4 * 32768 + wid * 4096u, wid);
  }
}

extern "C" void kernel_launch(void* const* d_in, const int* in_sizes, int n_in, void* d_out, int out_size, void* d_ws,
                              size_t ws_size, hipStream_t stream) {
  attn_fwd_1cu<<<dim3(NBH), dim3(256), 0, stream>>>((const uint16_t*)d_in[0], (const uint16_t*)d_in[1], (const uint16_t*)d_in[2],
                                              (uint16_t*)d_out);
}
```
